```python
import torch

def _softmax(x, axis=-1):
    return torch.softmax(x, dim=axis)
def _gelu(x):
    return torch.nn.functional.gelu(x, approximate="tanh")
def _silu(x):
    return torch.nn.functional.silu(x)
def _sigmoid(x):
    return torch.sigmoid(x)
def _layernorm(x, axis=-1, eps=1e-5):
    m = x.mean(dim=axis, keepdim=True)
    v = x.var(dim=axis, keepdim=True, unbiased=False)
    return (x - m) / torch.sqrt(v + eps)
def _scatter_add(tgt, idx, src, axis=0):
    return tgt.clone().index_add_(axis, idx, src)
def _segment_sum(src, seg, n):
    out = torch.zeros((n,) + src.shape[1:], dtype=src.dtype)
    return out.index_add_(0, seg, src)
def _pad(x, pw):
    flat = tuple(v for pair in reversed(pw) for v in pair)
    return torch.nn.functional.pad(x, flat)

kernel_name = 'interleave_layers__ssm_conv_s5__ssm_conv_h3__prompt'

def setup_inputs():

    _w = (lambda a: (lambda t: t.to(torch.bfloat16) if t.is_floating_point() else t)(torch.as_tensor(a)))
    def _A_setup():
        B, S, D = 1, 2048, 576
        P = D
        Lambda_re = -0.5 * (lambda _x: torch.exp(torch.as_tensor(_x)))((lambda *s: torch.randn(*s, dtype=torch.bfloat16) * 0.2)(P))
        Lambda_im = (lambda *s: torch.randn(*s, dtype=torch.bfloat16) * 0.4)(P) * 1.0
        B_re = (lambda *s: torch.randn(*s, dtype=torch.bfloat16) * 0.2)(P, D) * (D ** -0.5)
        B_im = (lambda *s: torch.randn(*s, dtype=torch.bfloat16) * 0.2)(P, D) * (D ** -0.5)
        C_re = (lambda *s: torch.randn(*s, dtype=torch.bfloat16) * 0.2)(D, P) * (P ** -0.5)
        C_im = (lambda *s: torch.randn(*s, dtype=torch.bfloat16) * 0.2)(D, P) * (P ** -0.5)
        log_dt = (lambda *s: torch.randn(*s, dtype=torch.bfloat16) * 0.2)(P) * 0.3 - 2.0
        Dskip = (lambda *s: torch.randn(*s, dtype=torch.bfloat16) * 0.05)(D)
        return {
            "x": _w((lambda *s: torch.randn(*s, dtype=torch.bfloat16) * 0.05)(B, S, D)),
            "Lambda_re": _w(Lambda_re),
            "Lambda_im": _w(Lambda_im),
            "B_re": _w(B_re),
            "B_im": _w(B_im),
            "C_re": _w(C_re),
            "C_im": _w(C_im),
            "log_dt": _w(log_dt),
            "Dskip": _w(Dskip),
        }
    _A_in = _A_setup()

    def _B_setup():
        B, S, D = 1, 2048, 576
        ks = 4
        return {
            "q": _w((lambda *s: torch.randn(*s, dtype=torch.bfloat16) * 0.05)(B, S, D)),
            "k": _w((lambda *s: torch.randn(*s, dtype=torch.bfloat16) * 0.4)(B, S, D)),
            "v": _w((lambda *s: torch.randn(*s, dtype=torch.bfloat16) * 0.4)(B, S, D)),
            "shift_k": _w((lambda *s: torch.randn(*s, dtype=torch.bfloat16) * 0.4)(D, ks) * 0.5),
            "ssm_k": _w((lambda *s: torch.randn(*s, dtype=torch.bfloat16) * 0.4)(D, S) * (1.0 / S ** 0.5)),
            "ssm_D": _w((lambda *s: torch.randn(*s, dtype=torch.bfloat16) * 0.4)(D)),
        }
    _B_in = _B_setup()

    return {
        "A_x": _A_in["x"],
        "A_Lambda_re": _A_in["Lambda_re"],
        "A_Lambda_im": _A_in["Lambda_im"],
        "A_B_re": _A_in["B_re"],
        "A_B_im": _A_in["B_im"],
        "A_C_re": _A_in["C_re"],
        "A_C_im": _A_in["C_im"],
        "A_log_dt": _A_in["log_dt"],
        "A_Dskip": _A_in["Dskip"],
        "B_q": _B_in["q"],
        "B_k": _B_in["k"],
        "B_v": _B_in["v"],
        "B_shift_k": _B_in["shift_k"],
        "B_ssm_k": _B_in["ssm_k"],
        "B_ssm_D": _B_in["ssm_D"],
    }

def reference(A_x, A_Lambda_re, A_Lambda_im, A_B_re, A_B_im, A_C_re, A_C_im, A_log_dt, A_Dskip, B_q, B_k, B_v, B_shift_k, B_ssm_k, B_ssm_D):
    def _A_ref(x, Lambda_re, Lambda_im, B_re, B_im, C_re, C_im, log_dt, Dskip):
        Bsz, L, H = x.shape
        Lam = Lambda_re + 1j * Lambda_im
        Bmat = B_re + 1j * B_im
        Cmat = C_re + 1j * C_im
        dt = torch.exp(log_dt)
        Lam_bar = torch.exp(dt * Lam)
        B_bar = ((Lam_bar - 1.0) / Lam)[:, None] * Bmat
        Bu = torch.einsum("bld,pd->blp", x + 0j, B_bar)
        h = torch.zeros_like(Bu[:, 0, :])
        hs = []
        for t in range(L):
            h = Lam_bar[None, :] * h + Bu[:, t, :]
            hs.append(h)
        hseq = torch.stack(hs, 1)
        y = 2.0 * torch.einsum("blp,dp->bld", hseq, Cmat).real + x * Dskip[None, None, :]
        return y

    def _B_ref(q, k, v, shift_k, ssm_k, ssm_D):
        B, S, D = q.shape
        ks = shift_k.shape[-1]
        k_pad = _pad(k, ((0, 0), (ks - 1, 0), (0, 0)))
        k_shift = torch.zeros_like(k)
        for t in range(ks):
            k_shift = k_shift + k_pad[:, t:t + S, :] * shift_k[:, ks - 1 - t][None, None, :]
        kv = k_shift * v
        L = 2 * S
        kv_pad = _pad(kv, ((0, 0), (0, S), (0, 0)))
        kf_pad = _pad(ssm_k, ((0, 0), (0, S)))
        kv_f = (lambda x, axes=(-2,-1): torch.fft.rfft2(x.float(), dim=axes))(kv_pad[:, None, :, :], axes=(1, 2))
        kf_f = (lambda x, axes=(-2,-1): torch.fft.rfft2(x.float(), dim=axes))(kf_pad[:, None, :], axes=(1, 2))
        kf_f = (lambda x, *d: x.permute(*d))(kf_f, 1, 2, 0)
        y_f = kv_f * kf_f[None, :, :, :]
        y = (lambda x, s, axes=(-2,-1): torch.fft.irfft2(x, s=s, dim=axes).to(torch.bfloat16))(y_f, (1, L), axes=(1, 2))[:, 0, :S, :]
        y = y + kv * ssm_D[None, None, :]
        return q * y

    x = A_x
    x = x + _A_ref(_layernorm(x, axis=-1), A_Lambda_re, A_Lambda_im, A_B_re, A_B_im, A_C_re, A_C_im, A_log_dt, A_Dskip)
    x = x + _B_ref(_layernorm(x, axis=-1), B_k, B_v, B_shift_k, B_ssm_k, B_ssm_D)
    x = x + _A_ref(_layernorm(x, axis=-1), A_Lambda_re, A_Lambda_im, A_B_re, A_B_im, A_C_re, A_C_im, A_log_dt, A_Dskip)
    x = x + _B_ref(_layernorm(x, axis=-1), B_k, B_v, B_shift_k, B_ssm_k, B_ssm_D)
    return x
```

```cpp
#include <hip/hip_runtime.h>
#include <hip/hip_bf16.h>
#include <stdint.h>

typedef __bf16 bf16x8 __attribute__((ext_vector_type(8)));
typedef __bf16 bf16x2 __attribute__((ext_vector_type(2)));
typedef float f32x4 __attribute__((ext_vector_type(4)));
typedef float f32x2 __attribute__((ext_vector_type(2)));
typedef unsigned u32x4 __attribute__((ext_vector_type(4)));
typedef unsigned u32x2 __attribute__((ext_vector_type(2)));
typedef unsigned short u16;
typedef __amdgpu_buffer_rsrc_t rsrc_t;
#define DEV __device__ __forceinline__

constexpr int SEQ = 2048, DM = 576, TC = 80, NP = 26, NG = 9;
constexpr unsigned WS_KVF = 4096;
constexpr unsigned WS_GF = WS_KVF + 9 * 256;
constexpr unsigned WS_HF = WS_GF + 9 * 256;
constexpr unsigned WS_XF = WS_HF + 2 * 26 * 128;
constexpr unsigned WS_BLK = 32768;
constexpr unsigned BLK = 4352, BLK_S1 = 1024, BLK_S2 = 3584, BLK_CH = 264;
constexpr unsigned WS_RKV = WS_BLK + 234 * BLK;
constexpr unsigned WS_GT = WS_RKV + DM * 4096;
constexpr unsigned WS_HT = WS_GT + DM * 4096;
constexpr unsigned WS_XT = WS_HT + 2 * 234 * 20480;
constexpr unsigned WS_END = WS_XT + 234 * 10240;
constexpr unsigned WS_STAMP = WS_END;
constexpr int L_K = 92160, L_R = 0, L_X = 16384, L_P = 40960, L_H = 81920, L_B = 122880, L_G = 143360, L_M = 158720;
constexpr int SPIN = 1 << 16;
constexpr unsigned SENT = 0xFFFFFFFFu;

#define BAR() asm volatile("s_waitcnt lgkmcnt(0)\n\ts_barrier" ::: "memory")
#define DRAIN() asm volatile("s_waitcnt vmcnt(0)" ::: "memory")
#define STAMP(i)

DEV unsigned pk(float lo, float hi) { f32x2 v = {lo, hi}; return __builtin_bit_cast(unsigned, __builtin_convertvector(v, bf16x2)); }
DEV float lo16(unsigned u) { return __uint_as_float(u << 16); }
DEV float hi16(unsigned u) { return __uint_as_float(u & 0xffff0000u); }
DEV float rb(float x) { return lo16(pk(x, x)); }
DEV float b2f(u16 h) { return __uint_as_float((unsigned)h << 16); }
template <int C> DEV float dpp(float v) { return __int_as_float(__builtin_amdgcn_update_dpp(0, __float_as_int(v), C, 0xf, 0xf, true)); }
DEV float dot2(unsigned a, unsigned b, float c) { return __builtin_amdgcn_fdot2_f32_bf16(__builtin_bit_cast(bf16x2, a), __builtin_bit_cast(bf16x2, b), c, false); }
DEV f32x4 mfma(u32x4 a, u32x4 b, f32x4 c) { return __builtin_amdgcn_mfma_f32_16x16x32_bf16(__builtin_bit_cast(bf16x8, a), __builtin_bit_cast(bf16x8, b), c, 0, 0, 0); }
DEV unsigned canon(float v) { unsigned u = __float_as_uint(v); return u == SENT ? 0x7fc00000u : u; }
struct cf { float r, i; };
DEV cf cmul(cf a, cf b) { return {a.r * b.r - a.i * b.i, a.r * b.i + a.i * b.r}; }
DEV cf cfma(cf a, cf b, cf c) { return {fmaf(a.r, b.r, fmaf(-a.i, b.i, c.r)), fmaf(a.r, b.i, fmaf(a.i, b.r, c.i))}; }
DEV cf csel(bool p, cf a, cf b) { return {p ? a.r : b.r, p ? a.i : b.i}; }
DEV u32x4 ld16(rsrc_t rs, unsigned v, unsigned s) { return __builtin_amdgcn_raw_buffer_load_b128(rs, v, s, 16); }
DEV u32x2 ld8(rsrc_t rs, unsigned v, unsigned s) { return __builtin_amdgcn_raw_buffer_load_b64(rs, v, s, 16); }
DEV unsigned ld4(rsrc_t rs, unsigned v, unsigned s) { return __builtin_amdgcn_raw_buffer_load_b32(rs, v, s, 16); }
DEV void st16(u32x4 d, rsrc_t rs, unsigned v, unsigned s) { __builtin_amdgcn_raw_buffer_store_b128(d, rs, v, s, 16); }
DEV void st8(u32x2 d, rsrc_t rs, unsigned v, unsigned s) { __builtin_amdgcn_raw_buffer_store_b64(d, rs, v, s, 16); }
DEV void st4(unsigned d, rsrc_t rs, unsigned v, unsigned s) { __builtin_amdgcn_raw_buffer_store_b32(d, rs, v, s, 16); }
DEV int wrap9(int v) { return v >= 9 ? v - 9 : v; }
DEV int fsw(int row) { return ((row / 20) << 1) | ((row >> 1) & 1); }

DEV void poll_flags(rsrc_t rs, unsigned off, int n, int lane, u32x2 tag) {
  unsigned vo = (lane < n ? lane : n - 1) * 8;
#pragma unroll 1
  for (int it = 0; it < SPIN; ++it) {
    u32x2 v = ld8(rs, vo, off);
    if (__all(v.x == tag.x && v.y == tag.y)) break;
    asm volatile("s_sleep 1" ::: "memory");
  }
  asm volatile("" ::: "memory");
}

DEV void build_k(char* lds, int tid, u32x4 lo, u32x4 hi) {
  unsigned w[8] = {lo.x, lo.y, lo.z, lo.w, hi.x, hi.y, hi.z, hi.w}, o[7];
#pragma unroll
  for (int k = 0; k < 7; ++k) o[k] = __builtin_amdgcn_alignbit(w[k + 1], w[k], 16);
  char* p = lds + L_K + (tid >> 8) * 33280 + ((tid & 255) + 1) * 16;
#pragma unroll
  for (int s = 0; s < 8; ++s) {
    u32x4 v;
    if (s & 1) v = u32x4{w[(s + 1) / 2], w[(s + 1) / 2 + 1], w[(s + 1) / 2 + 2], w[(s + 1) / 2 + 3]};
    else v = u32x4{o[s / 2], o[s / 2 + 1], o[s / 2 + 2], o[s / 2 + 3]};
    *(u32x4*)(p + s * 4160) = v;
  }
  if (tid < 16) *(u32x4*)(lds + L_K + (tid >> 3) * 33280 + (tid & 7) * 4160) = u32x4{0, 0, 0, 0};
}
DEV void load_k(const u16* ssk, int d0, int tid, u32x4& lo, u32x4& hi) {
  const u16* kr = ssk + (size_t)(d0 + (tid >> 8)) * SEQ;
  int ci = tid & 255;
  hi = *(const u32x4*)(kr + 8 * ci);
  lo = *(const u32x4*)(kr + 8 * (ci > 0 ? ci - 1 : 0));
}
template <int SET> DEV void conv_mfma(const char* kb, const char* ub, f32x4 (&Y)[4]) {
  constexpr int C0 = SET ? 1 : 0, C1 = SET ? 2 : 3, C2 = SET ? 4 : 5, C3 = SET ? 7 : 6;
#pragma unroll 2
  for (int rr = 0; rr < 4; ++rr) {
    u32x4 U[C3 + 1];
#pragma unroll
    for (int p = 0; p <= C3; ++p) U[p] = *(const u32x4*)(ub + 512 * (7 - p) + 64 * rr);
#pragma unroll
    for (int a = 0; a <= C3; ++a) {
      u32x4 K = *(const u32x4*)(kb + 512 * a + 64 * rr);
      Y[3] = mfma(K, U[C3 - a], Y[3]);
      if (C2 >= a) Y[2] = mfma(K, U[C2 >= a ? C2 - a : 0], Y[2]);
      if (C1 >= a) Y[1] = mfma(K, U[C1 >= a ? C1 - a : 0], Y[1]);
      if (C0 >= a) Y[0] = mfma(K, U[C0 >= a ? C0 - a : 0], Y[0]);
    }
  }
}
DEV void conv_pair(char* lds, rsrc_t rs, int d0, int tid, int w, const u16* sD) {
  int l = tid & 63, chn = w >> 2, set = w & 1, rh = (w >> 1) & 1;
  u32x4 rv = ld16(rs, (tid & 255) * 16, WS_RKV + (d0 + chn) * 4096);
  float dd = b2f(sD[d0 + chn]);
  if (tid < 60) *(u32x4*)(lds + L_R + (tid / 30) * 4608 + (256 + tid % 30) * 16) = u32x4{0, 0, 0, 0};
  *(u32x4*)(lds + L_R + chn * 4608 + (tid & 255) * 16) = rv;
  BAR();
  int b = l & 15, g = l >> 4;
  const char* kb = lds + L_K + chn * 33280 + (b & 7) * 4160 + (g + (b >> 3)) * 16 + rh * 256;
  const char* ub = lds + L_R + chn * 4608 + (254 - 2 * b + g) * 16 - 3584 + rh * 256;
  f32x4 Y[4] = {};
  if (set) conv_mfma<1>(kb, ub, Y); else conv_mfma<0>(kb, ub, Y);
  f32x4 keep[2];
#pragma unroll
  for (int k = 0; k < 2; ++k) {
    f32x4 snd;
#pragma unroll
    for (int m = 0; m < 4; ++m) {
      float a = Y[k][m], bb = Y[2 + k][m];
      asm volatile("" : "+v"(a), "+v"(bb));
      snd[m] = rh ? a : bb; keep[k][m] = rh ? bb : a;
    }
    *(f32x4*)(lds + L_X + w * 2048 + k * 1024 + l * 16) = snd;
  }
  BAR();
#pragma unroll
  for (int k = 0; k < 2; ++k) {
    f32x4 y = keep[k] + *(const f32x4*)(lds + L_X + (w ^ 2) * 2048 + k * 1024 + l * 16);
    int cb = ((set ? 0x7421 : 0x6530) >> (4 * (2 * rh + k))) & 15;
    int tb = 256 * cb + 16 * b + 4 * g;
    u32x2 kv = *(const u32x2*)(lds + L_R + chn * 4608 + (2044 - tb) * 2);
    unsigned y01 = pk(y.x, y.y), y23 = pk(y.z, y.w);
    unsigned s01 = pk(hi16(kv.y) * dd, lo16(kv.y) * dd), s23 = pk(hi16(kv.x) * dd, lo16(kv.x) * dd);
    u32x2 o = {pk(lo16(y01) + lo16(s01), hi16(y01) + hi16(s01)), pk(lo16(y23) + lo16(s23), hi16(y23) + hi16(s23))};
    st8(o, rs, tb * 2, WS_GT + (d0 + chn) * 4096);
  }
}
template <int I> DEV void g1slot(const char* lds, int ab, const u32x4 (&W1)[18], f32x4 (&acc)[5]) {
#pragma unroll
  for (int ks = 0; ks < 2; ++ks)
#pragma unroll
    for (int rt = 0; rt < 5; ++rt)
      acc[rt] = mfma(*(const u32x4*)(lds + (ab ^ (ks * 64)) + I * 10240 + rt * 512), W1[2 * I + ks], acc[rt]);
}
template <int I, int SLOT> DEV void g2tile(const char* lds, int hb, const u32x4 (&W2)[18], f32x4 (&acc)[5]) {
#pragma unroll
  for (int ab = 0; ab < 2; ++ab)
#pragma unroll
    for (int tt = 0; tt < 5; ++tt)
      acc[tt] = mfma(W2[2 * I + ab], *(const u32x4*)(lds + (hb ^ (ab * 16)) + SLOT * 20480 + tt * 4096), acc[tt]);
}
template <int PER> DEV void ld_tiles(rsrc_t rs, unsigned base, int g, int i0, int w, int l, u32x4 (&v)[5]) {
#pragma unroll
  for (int n = 0; n < 5; ++n) {
    int pc = w + 8 * n, tl = pc / PER;
    v[n] = ld16(rs, l * 16, base + wrap9(g + i0 + tl) * (PER * 1024) + (pc - tl * PER) * 1024);
  }
}
DEV void wr_tiles(char* dst, int tid, const u32x4 (&v)[5]) {
#pragma unroll
  for (int n = 0; n < 5; ++n) *(u32x4*)(dst + tid * 16 + n * 8192) = v[n];
}

__global__ __launch_bounds__(512, 2) void fused_kernel(
    const u16* __restrict__ X, const u16* __restrict__ Lre, const u16* __restrict__ Lim, const u16* __restrict__ Bre,
    const u16* __restrict__ Bim, const u16* __restrict__ Cre, const u16* __restrict__ Cim, const u16* __restrict__ logdt,
    const u16* __restrict__ Dsk, const u16* __restrict__ Kin, const u16* __restrict__ Vin, const u16* __restrict__ shk,
    const u16* __restrict__ ssk, const u16* __restrict__ sD, float* __restrict__ out, char* ws) {
  __shared__ __attribute__((aligned(16))) char lds[163840];
  int tid = threadIdx.x, l = tid & 63;
  const int w = __builtin_amdgcn_readfirstlane(tid >> 6);
  const int xcd = blockIdx.x & 7, loc = blockIdx.x >> 3;
  int c, g, spare = -1;
  if (loc < 27) { c = 3 * xcd + loc / 9; g = loc % 9; }
  else {
    int idx = xcd * 5 + loc - 27;
    if (idx >= 36) return;
    if (idx < 18) { c = 24 + idx / 9; g = idx % 9; } else { spare = idx - 18; c = 0; g = spare >> 1; }
  }
  asm volatile("" : "+s"(ws));
  const rsrc_t rs = __builtin_amdgcn_make_buffer_rsrc(ws, 0, WS_END, 0x00020000);
  STAMP(0);
  u32x2 tag = ld8(rs, 0, 0);
  if (spare >= 0) {
    tag.x += 1; tag.y += 1;
#pragma unroll 1
    for (int r = 0; r < 3; ++r) {
      int d0 = 64 * g + 2 * (26 + 3 * (spare & 1) + r);
      u32x4 klo, khi;
      load_k(ssk, d0, tid, klo, khi);
      if ((tid & 255) == 0) klo = u32x4{0, 0, 0, 0};
      build_k(lds, tid, klo, khi);
      if (r == 0) poll_flags(rs, WS_KVF + g * 256, NP, l, tag);
      conv_pair(lds, rs, d0, tid, w, sD);
      BAR();
    }
    DRAIN();
    BAR();
    if (tid == 0) st8(tag, rs, 0, WS_GF + g * 256 + (26 + (spare & 1)) * 8);
    STAMP(1);
    return;
  }
  const int t0 = TC * c, me = c * 9 + g;
  int q = l >> 4, ri = l & 1, pp = (l & 15) >> 1, s8 = 2 * q + ri, er, ei, eo0;
#define REDERIVE() do { asm volatile("" : "+v"(tid)); l = tid & 63; q = l >> 4; ri = l & 1; pp = (l & 15) >> 1; s8 = 2 * q + ri; \
    er = tid >> 2; ei = l & 3; eo0 = er * 128 + ((ei ^ fsw(er)) << 4); } while (0)
  unsigned kk[11], vv[8]; u32x4 shv;
  const int dp = l & 31, tb = t0 + 8 * (2 * w + (l >> 5));
  if (w < 5) {
#pragma unroll
    for (int j = 0; j < 11; ++j) { int r = min(max(tb - 3 + j, 0), SEQ - 1); kk[j] = *(const unsigned*)(Kin + r * DM + 64 * g + 2 * dp); }
#pragma unroll
    for (int j = 0; j < 8; ++j) { int r = min(tb + j, SEQ - 1); vv[j] = *(const unsigned*)(Vin + r * DM + 64 * g + 2 * dp); }
    shv = *(const u32x4*)(shk + (64 * g + 2 * dp) * 4);
  }
  u32x4 xa[9], xb[3];
  const int row = tid >> 3, row2 = 64 + (tid >> 5), e = tid & 31;
  {
    const u16* xr = X + min(t0 + row, SEQ - 1) * DM + (tid & 7) * 8;
#pragma unroll
    for (int i = 0; i < 9; ++i) xa[i] = *(const u32x4*)(xr + wrap9(g + i) * 64);
    const u16* xr2 = X + min(t0 + row2, SEQ - 1) * DM + (e & 7) * 8;
#pragma unroll
    for (int n = 0; n < 3; ++n) xb[n] = *(const u32x4*)(xr2 + wrap9(g + min((e >> 3) + 4 * n, 8)) * 64);
  }
  u32x4 klo, khi;
  load_k(ssk, 64 * g + 2 * c, tid, klo, khi);
  const int p = 64 * g + 8 * w + pp;
  const float lre = b2f(Lre[p]), lim = b2f(Lim[p]), ldt = b2f(logdt[p]);
  u32x4 W1[18];
  {
    const u16* wb = (ri ? Bim : Bre) + p * DM + 8 * q;
#pragma unroll
    for (int i = 0; i < 9; ++i) {
      W1[2 * i] = *(const u32x4*)(wb + wrap9(g + i) * 64);
      W1[2 * i + 1] = *(const u32x4*)(wb + wrap9(g + i) * 64 + 32);
    }
  }
  if (tid < BLK_CH) st16(u32x4{SENT, SENT, SENT, SENT}, rs, tid * 16, WS_BLK + me * BLK);
  if (w < 5) {
    float sa[4] = {lo16(shv.x), hi16(shv.x), lo16(shv.y), hi16(shv.y)}, sb[4] = {lo16(shv.z), hi16(shv.z), lo16(shv.w), hi16(shv.w)};
    unsigned P[8];
#pragma unroll
    for (int j = 0; j < 3; ++j) if (tb - 3 + j < 0) kk[j] = 0;
#pragma unroll
    for (int j = 0; j < 8; ++j) {
      unsigned a = pk(lo16(kk[j]) * sa[3], hi16(kk[j]) * sb[3]);
#pragma unroll
      for (int tt = 1; tt < 4; ++tt) {
        unsigned m = pk(lo16(kk[j + tt]) * sa[3 - tt], hi16(kk[j + tt]) * sb[3 - tt]);
        a = pk(lo16(a) + lo16(m), hi16(a) + hi16(m));
      }
      P[j] = pk(lo16(a) * lo16(vv[j]), hi16(a) * hi16(vv[j]));
    }
    if (tb < SEQ) {
      u32x4 o0, o1;
#pragma unroll
      for (int k = 0; k < 4; ++k) {
        o0[k] = __builtin_amdgcn_perm(P[6 - 2 * k], P[7 - 2 * k], 0x05040100);
        o1[k] = __builtin_amdgcn_perm(P[6 - 2 * k], P[7 - 2 * k], 0x07060302);
      }
      unsigned vo = 2 * dp * 4096 + (2040 - tb) * 2;
      st16(o0, rs, vo, WS_RKV + 64 * g * 4096);
      st16(o1, rs, vo + 4096, WS_RKV + 64 * g * 4096);
    }
  }
  STAMP(17);
  {
    float s1 = 0.f, s2 = 0.f, u1 = 0.f, u2 = 0.f;
    char* d = lds + row * 128 + (((tid & 7) ^ fsw(row)) << 4);
#pragma unroll
    for (int i = 0; i < 9; ++i) {
      *(u32x4*)(d + i * 10240) = xa[i];
#pragma unroll
      for (int k = 0; k < 4; ++k) { s1 = dot2(xa[i][k], 0x3f803f80u, s1); s2 = dot2(xa[i][k], xa[i][k], s2); }
    }
    char* d2 = lds + row2 * 128 + (((e & 7) ^ fsw(row2)) << 4) + (e >> 3) * 10240;
#pragma unroll
    for (int n = 0; n < 3; ++n)
      if (n < 2 || e < 8) {
        *(u32x4*)(d2 + n * 40960) = xb[n];
#pragma unroll
        for (int k = 0; k < 4; ++k) { u1 = dot2(xb[n][k], 0x3f803f80u, u1); u2 = dot2(xb[n][k], xb[n][k], u2); }
      }
    s1 += dpp<0xB1>(s1); s2 += dpp<0xB1>(s2); u1 += dpp<0xB1>(u1); u2 += dpp<0xB1>(u2);
    s1 += dpp<0x4E>(s1); s2 += dpp<0x4E>(s2); u1 += dpp<0x4E>(u1); u2 += dpp<0x4E>(u2);
    s1 += dpp<0x141>(s1); s2 += dpp<0x141>(s2); u1 += dpp<0x141>(u1); u2 += dpp<0x141>(u2);
    u1 += dpp<0x140>(u1); u2 += dpp<0x140>(u2);
    u1 += __shfl_xor(u1, 16); u2 += __shfl_xor(u2, 16);
    bool ex = (tid & 31) == 1;
    float a1 = (ex ? u1 : s1) * (1.f / DM), a2 = (ex ? u2 : s2) * (1.f / DM);
    float var = fmaxf(a2 - a1 * a1, 0.f), m = rb(a1), sd = rb(sqrtf(rb(rb(var) + 1e-5f)));
    if (ex || (tid & 7) == 0) {
      int r = ex ? row2 : row;
      *(f32x2*)(lds + L_M + r * 8) = f32x2{m, 1.f / sd};
      *(float*)(lds + L_M + 640 + r * 4) = sd;
    }
  }
  STAMP(18);
  if ((tid & 255) == 0) klo = u32x4{0, 0, 0, 0};
  build_k(lds, tid, klo, khi);
  STAMP(19);
  cf lam, co2;
  {
    float dt = rb(expf(ldt)), er = expf(dt * lre), sn, cs;
    sincosf(dt * lim, &sn, &cs);
    lam = {er * cs, er * sn};
    float den = 2.f / (lre * lre + lim * lim), nr = lam.r - 1.f, ni = lam.i;
    co2 = {(nr * lre + ni * lim) * den, (ni * lre - nr * lim) * den};
  }
  tag.x += 1; tag.y += 1;
  DRAIN();
  BAR();
  if (tid == 0) st8(tag, rs, 0, WS_KVF + g * 256 + c * 8);
  STAMP(1);
  float csr, csi;
  {
    float cs = 0.f;
#pragma unroll
    for (int i = 0; i < 18; ++i)
#pragma unroll
      for (int k = 0; k < 4; ++k) cs = dot2(W1[i][k], 0x3f803f80u, cs);
    cs += __shfl_xor(cs, 16); cs += __shfl_xor(cs, 32);
    float o = dpp<0xB1>(cs);
    csr = ri ? o : cs; csi = ri ? cs : o;
  }
  u32x4 W2[18];
  float piv = 0.f;

#pragma unroll
  for (int L = 0; L < 2; ++L) {
    f32x4 acc[5] = {};
    REDERIVE();
    const int ab = (20 * ((l & 15) >> 2) + (l & 3)) * 128 + ((q ^ ((l & 15) >> 1)) << 4);
    cf l10, l20, l10s, l80s, l640;
    asm volatile("" : "+v"(lam.r), "+v"(lam.i));
    {
    cf l2 = cmul(lam, lam), l4 = cmul(l2, l2), l5 = cmul(l4, lam), one = {1.f, 0.f};
    l10 = cmul(l5, l5); l20 = cmul(l10, l10);
    cf l40 = cmul(l20, l20), l80 = cmul(l40, l40), l160 = cmul(l80, l80), l320 = cmul(l160, l160);
    l640 = cmul(l320, l320);
    l10s = cmul(cmul(csel(ri, l10, one), csel(q & 1, l20, one)), csel(q & 2, l40, one));
    l80s = cmul(cmul(csel(ri, l80, one), csel(q & 1, l160, one)), csel(q & 2, l320, one));
    }
    if (L == 0) {
      g1slot<0>(lds, ab, W1, acc); g1slot<1>(lds, ab, W1, acc); g1slot<2>(lds, ab, W1, acc); g1slot<3>(lds, ab, W1, acc);
      g1slot<4>(lds, ab, W1, acc); g1slot<5>(lds, ab, W1, acc); g1slot<6>(lds, ab, W1, acc); g1slot<7>(lds, ab, W1, acc);
      g1slot<8>(lds, ab, W1, acc);
    } else {
      u32x4 ta[5], tc[5];
      poll_flags(rs, WS_XF + c * 128, NG, l, tag);
      STAMP(10);
      ld_tiles<10>(rs, WS_XT + c * 9 * 10240, g, 1, w, l, ta);
      ld_tiles<10>(rs, WS_XT + c * 9 * 10240, g, 5, w, l, tc);
      g1slot<0>(lds, ab, W1, acc);
      wr_tiles(lds + 10240, tid, ta);
      BAR();
      g1slot<1>(lds, ab, W1, acc); g1slot<2>(lds, ab, W1, acc);
      wr_tiles(lds + 5 * 10240, tid, tc);
      g1slot<3>(lds, ab, W1, acc); g1slot<4>(lds, ab, W1, acc);
      BAR();
      g1slot<5>(lds, ab, W1, acc); g1slot<6>(lds, ab, W1, acc); g1slot<7>(lds, ab, W1, acc); g1slot<8>(lds, ab, W1, acc);
    }
    STAMP(2 + 9 * L);
    REDERIVE();
    cf u[10], hin;
    {
#pragma unroll
      for (int j = 0; j < 10; ++j) {
        float lo = acc[j >> 2][j & 3], hi = acc[(10 + j) >> 2][(10 + j) & 3];
        float a = dpp<0xB1>(lo), b = dpp<0xB1>(hi);
        cf v = {ri ? b : lo, ri ? hi : a};
        if (L == 0) {
          f32x2 ms = *(const f32x2*)(lds + L_M + (10 * s8 + j) * 8);
          v = {(v.r - ms.x * csr) * ms.y, (v.i - ms.x * csi) * ms.y};
        }
        u[j] = cmul(co2, v);
      }
      cf h = u[0];
#pragma unroll
      for (int j = 1; j < 10; ++j) h = cfma(lam, h, u[j]);
      cf o = {dpp<0xB1>(h.r), dpp<0xB1>(h.i)};
      cf ev = csel(ri, o, h), od = csel(ri, h, o);
      cf P = cfma(l10, ev, od), Pq[4];
#pragma unroll
      for (int k = 0; k < 4; ++k) Pq[k] = {__shfl(P.r, (l & 15) + 16 * k), __shfl(P.i, (l & 15) + 16 * k)};
      cf S2 = cfma(l20, Pq[0], Pq[1]), S3 = cfma(l20, S2, Pq[2]), E = cfma(l20, S3, Pq[3]);
      if (s8 == 0) st8(u32x2{canon(E.r), canon(E.i)}, rs, (8 * w + pp) * 8, WS_BLK + me * BLK + L * 512);
      cf Sq = csel(q & 2, csel(q & 1, S3, S2), csel(q & 1, Pq[0], cf{0.f, 0.f}));
      hin = csel(ri, cfma(l10, Sq, ev), Sq);
    }
    if (L == 0) {
      BAR();
      STAMP(26);
      poll_flags(rs, WS_KVF + g * 256, NP, l, tag);
      STAMP(3);
      conv_pair(lds, rs, 64 * g + 2 * c, tid, w, sD);
      if (w < 5) {
        const u16* xp = X + min(t0 + er, SEQ - 1) * DM + 64 * g + 8 * ei;
        u32x4 a = *(const u32x4*)xp, b = *(const u32x4*)(xp + 32);
        u32x4 dsa = *(const u32x4*)(Dsk + 64 * g + 8 * ei), dsb = *(const u32x4*)(Dsk + 64 * g + 32 + 8 * ei);
        float m = *(const float*)(lds + L_M + er * 8), sd = *(const float*)(lds + L_M + 640 + er * 4), base[16];
        piv = m;
#pragma unroll
        for (int k = 0; k < 8; ++k) {
          unsigned xx = k < 4 ? a[k & 3] : b[k & 3], ds = k < 4 ? dsa[k & 3] : dsb[k & 3];
          float x0 = lo16(xx) - m, x1 = hi16(xx) - m;
          unsigned r = pk(x0, x1);
          r = pk(lo16(r) / sd, hi16(r) / sd);
          r = pk(lo16(r) * lo16(ds), hi16(r) * hi16(ds));
          base[2 * k] = x0 + lo16(r); base[2 * k + 1] = x1 + hi16(r);
        }
#pragma unroll
        for (int k = 0; k < 4; ++k)
          *(f32x4*)(lds + L_B + k * 5120 + tid * 16) = f32x4{base[4 * k], base[4 * k + 1], base[4 * k + 2], base[4 * k + 3]};
      }
      DRAIN();
      BAR();
      if (tid == 0) st8(tag, rs, 0, WS_GF + g * 256 + c * 8);
      STAMP(4);
    } else {
      BAR();
    }
    REDERIVE();
    if (c > 0) {
      u32x2 ev[4];
#pragma unroll 1
      for (int it = 0; it < SPIN; ++it) {
        bool ok = true;
#pragma unroll
        for (int i = 0; i < 4; ++i) {
          int cp = max(c - 1 - s8 - 8 * i, 0);
          ev[i] = ld8(rs, cp * (9 * BLK) + (8 * w + pp) * 8, WS_BLK + g * BLK + L * 512);
          ok = ok && ev[i].x != SENT && ev[i].y != SENT;
        }
        if (__all(ok)) break;
        asm volatile("s_sleep 1" ::: "memory");
      }
      cf H = {0.f, 0.f};
#pragma unroll
      for (int i = 3; i >= 0; --i) {
        bool val = c - 1 - s8 - 8 * i >= 0;
        H = cfma(l640, H, cf{val ? __uint_as_float(ev[i].x) : 0.f, val ? __uint_as_float(ev[i].y) : 0.f});
      }
      H = cmul(l80s, H);
      H.r += dpp<0xB1>(H.r); H.i += dpp<0xB1>(H.i);
      H.r += __shfl_xor(H.r, 16); H.i += __shfl_xor(H.i, 16);
      H.r += __shfl_xor(H.r, 32); H.i += __shfl_xor(H.i, 32);
      hin = cfma(l10s, H, hin);
    }
    STAMP(5 + 7 * L);
#pragma unroll
    for (int j = 0; j < 10; ++j) {
      hin = cfma(lam, hin, u[j]);
      int t = 10 * s8 + j;
      *(unsigned*)(lds + L_H + t * 256 + (((2 * w + (pp >> 2)) ^ (t & 15)) << 4) + (pp & 3) * 4) = pk(hin.r, hin.i);
    }
    BAR();
    const unsigned hbase = WS_HT + (L * NP + c) * 9 * 20480;
#pragma unroll
    for (int n = 0; n < 3; ++n)
      if (n < 2 || w < 4) st16(*(const u32x4*)(lds + L_H + tid * 16 + n * 8192), rs, tid * 16 + n * 8192, hbase + g * 20480);
    DRAIN();
    BAR();
    if (tid == 0) st8(tag, rs, 0, WS_HF + (L * NP + c) * 128 + g * 8);
    STAMP(6 + 7 * L);
    u32x4 gv[2];
    if (L == 0) {
      const int dro = (64 * g + 16 * (w & 3) + (l & 15)) * DM + 32 * (w >> 2) + 8 * q;
#pragma unroll
      for (int i = 0; i < 9; ++i) {
        W2[2 * i] = *(const u32x4*)(Cre + dro + wrap9(g + i) * 64);
        W2[2 * i + 1] = *(const u32x4*)(Cim + dro + wrap9(g + i) * 64);
      }
      poll_flags(rs, WS_GF + g * 256, 28, l, tag);
#pragma unroll
      for (int n = 0; n < 2; ++n) {
        int id = min(tid + 512 * n, 639), dl = id / 10, ck = id - 10 * dl;
        gv[n] = ld16(rs, dl * 4096 + min(t0 + 8 * ck, SEQ - 8) * 2, WS_GT + 64 * g * 4096);
      }
#pragma unroll
      for (int i = 0; i < 9; ++i) {
        u32x4 re = W2[2 * i], im = W2[2 * i + 1] ^ 0x80008000u;
        W2[2 * i] = u32x4{__builtin_amdgcn_perm(im.x, re.x, 0x05040100), __builtin_amdgcn_perm(im.x, re.x, 0x07060302),
                          __builtin_amdgcn_perm(im.y, re.y, 0x05040100), __builtin_amdgcn_perm(im.y, re.y, 0x07060302)};
        W2[2 * i + 1] = u32x4{__builtin_amdgcn_perm(im.z, re.z, 0x05040100), __builtin_amdgcn_perm(im.z, re.z, 0x07060302),
                              __builtin_amdgcn_perm(im.w, re.w, 0x05040100), __builtin_amdgcn_perm(im.w, re.w, 0x07060302)};
      }
#pragma unroll
      for (int n = 0; n < 2; ++n) {
        int id = min(tid + 512 * n, 639), dl = id / 10;
        *(u32x4*)(lds + L_G + id * 16 + ((dl >> 3) & 3) * 32 + (dl >> 5) * 128) = gv[n];
      }
    }
    unsigned Gp[8];
    u32x4 dsa, dsb;
    f32x4 base[4];
    {
      f32x4 y[5] = {};
      REDERIVE();
      const int hb = (l & 15) * 256 + (((8 * (w >> 2) + 2 * q) ^ (l & 15)) << 4);
      u32x4 ta[5], tc[5];
      poll_flags(rs, WS_HF + (L * NP + c) * 128, NG, l, tag);
      STAMP(7 + 7 * L);
      ld_tiles<20>(rs, hbase, g, 1, w, l, ta);
      ld_tiles<20>(rs, hbase, g, 3, w, l, tc);
      g2tile<0, 4>(lds, hb, W2, y);
      wr_tiles(lds, tid, ta);
      ld_tiles<20>(rs, hbase, g, 5, w, l, ta);
      BAR();
      if (L) STAMP(20);
      g2tile<1, 0>(lds, hb, W2, y);
      wr_tiles(lds + 2 * 20480, tid, tc);
      ld_tiles<20>(rs, hbase, g, 7, w, l, tc);
      g2tile<2, 1>(lds, hb, W2, y);
      BAR();
      if (L) STAMP(21);
      g2tile<3, 2>(lds, hb, W2, y);
      wr_tiles(lds + 4 * 20480, tid, ta);
      g2tile<4, 3>(lds, hb, W2, y);
      BAR();
      if (L) STAMP(22);
      g2tile<5, 4>(lds, hb, W2, y);
      wr_tiles(lds, tid, tc);
      g2tile<6, 5>(lds, hb, W2, y);
      BAR();
      if (L) STAMP(23);
      REDERIVE();
      if (L == 0 && w < 5) { dsa = *(const u32x4*)(Dsk + 64 * g + 8 * ei); dsb = *(const u32x4*)(Dsk + 64 * g + 32 + 8 * ei); }
      g2tile<7, 0>(lds, hb, W2, y);
      g2tile<8, 1>(lds, hb, W2, y);
#pragma unroll
      for (int tt = 0; tt < 5; ++tt)
        *(f32x4*)(lds + L_P + (w >> 2) * 21760 + (16 * tt + (l & 15)) * 272 + (16 * (w & 3) + 4 * q) * 4) = y[tt];
      if (w < 5) {
#pragma unroll
        for (int k = 0; k < 4; ++k) base[k] = *(const f32x4*)(lds + L_B + k * 5120 + tid * 16);
        const char* gp = lds + L_G + 8 * ei * 160 + ei * 32 + er * 2;
#pragma unroll
        for (int k = 0; k < 8; ++k) {
          int o = (k >> 2) * (32 * 160 + 128) + (k & 3) * 320;
          Gp[k] = (unsigned)*(const u16*)(gp + o) | ((unsigned)*(const u16*)(gp + o + 160) << 16);
        }
      }
      BAR();
    }
    STAMP(8 + 7 * L);
    if (w < 5) {
      float z[16];
      const char* pb = lds + L_P + er * 272 + ei * 32;
#pragma unroll
      for (int k = 0; k < 4; ++k) {
        int o = (k >> 1) * 128 + (k & 1) * 16;
        f32x4 a = *(const f32x4*)(pb + o), b = *(const f32x4*)(pb + 21760 + o);
#pragma unroll
        for (int m = 0; m < 4; ++m) z[4 * k + m] = base[k][m] + (a[m] + b[m]);
      }
      if (L == 0) {
        float s[8] = {};
#pragma unroll
        for (int k = 0; k < 16; ++k) {
          float G = (k & 1) ? hi16(Gp[k >> 1]) : lo16(Gp[k >> 1]), zg = z[k] * G;
          s[0] += z[k]; s[1] = fmaf(z[k], z[k], s[1]); s[2] += zg; s[3] = fmaf(z[k], zg, s[3]);
          s[4] = fmaf(zg, zg, s[4]); s[5] = fmaf(zg, G, s[5]); s[6] += G; s[7] = fmaf(G, G, s[7]);
        }
#pragma unroll
        for (int k = 0; k < 8; ++k) { s[k] += dpp<0xB1>(s[k]); s[k] += dpp<0x4E>(s[k]); }
        float v0 = (ei & 2) ? ((ei & 1) ? s[6] : s[4]) : ((ei & 1) ? s[2] : s[0]);
        float v1 = (ei & 2) ? ((ei & 1) ? s[7] : s[5]) : ((ei & 1) ? s[3] : s[1]);
        st8(u32x2{canon(v0), canon(v1)}, rs, tid * 8, WS_BLK + me * BLK + BLK_S1);
        STAMP(24);
        u32x2 sv[9];
#pragma unroll 1
        for (int it = 0; it < SPIN; ++it) {
          bool ok = true;
#pragma unroll
          for (int k = 0; k < 9; ++k) { sv[k] = ld8(rs, tid * 8, WS_BLK + (c * 9 + k) * BLK + BLK_S1); ok = ok && sv[k].x != SENT && sv[k].y != SENT; }
          if (__all(ok)) break;
          asm volatile("s_sleep 1" ::: "memory");
        }
        STAMP(25);
        v0 = 0.f; v1 = 0.f;
#pragma unroll
        for (int k = 0; k < 9; ++k) { v0 += __uint_as_float(sv[k].x); v1 += __uint_as_float(sv[k].y); }
        v0 *= 1.f / DM; v1 *= 1.f / DM;
        float mu = dpp<0x00>(v0), zz = dpp<0x00>(v1), zG = dpp<0x55>(v0), zzG = dpp<0x55>(v1);
        float zzGG = dpp<0xAA>(v0), zGG = dpp<0xAA>(v1), G1 = dpp<0xFF>(v0), G2 = dpp<0xFF>(v1);
        float a = rsqrtf(fmaxf(zz - mu * mu, 0.f) + 1e-5f);
        float mw = mu + a * (zG - mu * G1);
        float ww = zz + 2.f * a * (zzG - mu * zG) + a * a * (zzGG - 2.f * mu * zGG + mu * mu * G2);
        float a2 = rsqrtf(fmaxf(ww - mw * mw, 0.f) + 1e-5f);
        piv += mw;
        u32x4 o0, o1;
#pragma unroll
        for (int k = 0; k < 8; ++k) {
          unsigned ds = k < 4 ? dsa[k & 3] : dsb[k & 3];
          float w0 = fmaf((z[2 * k] - mu) * a, lo16(Gp[k]), z[2 * k]) - mw, w1 = fmaf((z[2 * k + 1] - mu) * a, hi16(Gp[k]), z[2 * k + 1]) - mw;
          float n0 = w0 * a2, n1 = w1 * a2;
          base[k >> 1][(2 * k) & 3] = fmaf(n0, lo16(ds), w0); base[k >> 1][(2 * k + 1) & 3] = fmaf(n1, hi16(ds), w1);
          if (k < 4) o0[k & 3] = pk(n0, n1); else o1[k & 3] = pk(n0, n1);
        }
        st16(o0, rs, eo0, WS_XT + me * 10240);
        st16(o1, rs, eo0 ^ 64, WS_XT + me * 10240);
        *(u32x4*)(lds + eo0) = o0;
        *(u32x4*)(lds + (eo0 ^ 64)) = o1;
#pragma unroll
        for (int k = 0; k < 4; ++k) *(f32x4*)(lds + L_B + k * 5120 + tid * 16) = base[k];
        DRAIN();
      } else {
        float s0 = 0.f, s1 = 0.f;
#pragma unroll
        for (int k = 0; k < 16; ++k) { s0 += z[k]; s1 = fmaf(z[k], z[k], s1); }
        s0 += dpp<0xB1>(s0); s1 += dpp<0xB1>(s1); s0 += dpp<0x4E>(s0); s1 += dpp<0x4E>(s1);
        if (ei == 0) st8(u32x2{canon(s0), canon(s1)}, rs, er * 8, WS_BLK + me * BLK + BLK_S2);
        u32x2 sv[3];
#pragma unroll 1
        for (int it = 0; it < SPIN; ++it) {
          bool ok = true;
#pragma unroll
          for (int k = 0; k < 3; ++k) { sv[k] = ld8(rs, er * 8 + min(ei + 4 * k, 8) * BLK, WS_BLK + c * 9 * BLK + BLK_S2); ok = ok && sv[k].x != SENT && sv[k].y != SENT; }
          if (__all(ok)) break;
          asm volatile("s_sleep 1" ::: "memory");
        }
        s0 = __uint_as_float(sv[0].x) + __uint_as_float(sv[1].x); s1 = __uint_as_float(sv[0].y) + __uint_as_float(sv[1].y);
        s0 += dpp<0xB1>(s0); s1 += dpp<0xB1>(s1); s0 += dpp<0x4E>(s0); s1 += dpp<0x4E>(s1);
        s0 += __uint_as_float(sv[2].x); s1 += __uint_as_float(sv[2].y);
        float mu = s0 * (1.f / DM), a = rsqrtf(fmaxf(s1 * (1.f / DM) - mu * mu, 0.f) + 1e-5f);
        if (t0 + er < SEQ) {
          float* op = out + (t0 + er) * DM + 64 * g + 8 * ei;
#pragma unroll
          for (int k = 0; k < 4; ++k) {
            f32x4 o;
#pragma unroll
            for (int m = 0; m < 4; ++m) {
              int id = 4 * k + m;
              float G = (id & 1) ? hi16(Gp[id >> 1]) : lo16(Gp[id >> 1]);
              o[m] = fmaf((z[id] - mu) * a, G, z[id] + piv);
            }
            *(f32x4*)(op + (k >> 1) * 32 + (k & 1) * 4) = o;
          }
        }
      }
    }
    if (L == 0) {
      BAR();
      if (tid == 0) st8(tag, rs, 0, WS_XF + c * 128 + g * 8);
      STAMP(9);
    }
  }
  if (me == 25 * 9 && tid == 0) st8(tag, rs, 0, 0);
  STAMP(16);
}

extern "C" void kernel_launch(void* const* d_in, const int* in_sizes, int n_in, void* d_out, int out_size, void* d_ws,
                              size_t ws_size, hipStream_t stream) {
  const u16* const* in = (const u16* const*)d_in;
  fused_kernel<<<256, 512, 0, stream>>>(in[0], in[1], in[2], in[3], in[4], in[5], in[6], in[7], in[8], in[10], in[11],
                                        in[12], in[13], in[14], (float*)d_out, (char*)d_ws);
}
```

```cpp
#include <hip/hip_runtime.h>
#include <hip/hip_bf16.h>
#include <stdint.h>

typedef __bf16 bf16x8 __attribute__((ext_vector_type(8)));
typedef __bf16 bf16x2 __attribute__((ext_vector_type(2)));
typedef float f32x4 __attribute__((ext_vector_type(4)));
typedef float f32x2 __attribute__((ext_vector_type(2)));
typedef unsigned u32x4 __attribute__((ext_vector_type(4)));
typedef unsigned u32x2 __attribute__((ext_vector_type(2)));
typedef unsigned short u16;
typedef __amdgpu_buffer_rsrc_t rsrc_t;
#define DEV __device__ __forceinline__

constexpr int SEQ = 2048, DM = 576, TC = 80, NP = 26, NG = 9;
constexpr unsigned WS_KVF = 4096;
constexpr unsigned WS_GF = WS_KVF + 9 * 256;
constexpr unsigned WS_BLK = 32768;
constexpr unsigned BLK = 4352, BLK_S1 = 1024, BLK_S2 = 3584, BLK_CH = 264;
constexpr unsigned WS_RKV = WS_BLK + 234 * BLK;
constexpr unsigned WS_GT = WS_RKV + DM * 4096;
constexpr unsigned WS_HT = WS_GT + DM * 4096;
constexpr unsigned WS_XT = WS_HT + 2 * 234 * 20480;
constexpr unsigned WS_STAMP = WS_XT + 234 * 10240;
constexpr unsigned WS_W2 = WS_STAMP + 65536;
constexpr unsigned WS_END = WS_W2 + 81 * 16384;
constexpr unsigned RB = DM * 2, OOB = 0x80000000u;
constexpr int L_K = 92160, L_R = 0, L_X = 16384, L_P = 40960, L_H = 81920, L_B = 122880, L_G = 143360, L_M = 158720;
constexpr int SPIN = 1 << 16;
constexpr unsigned SENT = 0xFFFFFFFFu;

#define BAR() asm volatile("s_waitcnt lgkmcnt(0)\n\ts_barrier" ::: "memory")
#define DRAIN() asm volatile("s_waitcnt vmcnt(0)" ::: "memory")
#define STAMP(i)
#define DUMP()

DEV unsigned pk(float lo, float hi) { f32x2 v = {lo, hi}; return __builtin_bit_cast(unsigned, __builtin_convertvector(v, bf16x2)); }
DEV float lo16(unsigned u) { return __uint_as_float(u << 16); }
DEV float hi16(unsigned u) { return __uint_as_float(u & 0xffff0000u); }
DEV float rb(float x) { return lo16(pk(x, x)); }
DEV float b2f(u16 h) { return __uint_as_float((unsigned)h << 16); }
template <int C> DEV float dpp(float v) { return __int_as_float(__builtin_amdgcn_update_dpp(0, __float_as_int(v), C, 0xf, 0xf, true)); }
DEV float dot2(unsigned a, unsigned b, float c) { return __builtin_amdgcn_fdot2_f32_bf16(__builtin_bit_cast(bf16x2, a), __builtin_bit_cast(bf16x2, b), c, false); }
DEV f32x4 mfma(u32x4 a, u32x4 b, f32x4 c) { return __builtin_amdgcn_mfma_f32_16x16x32_bf16(__builtin_bit_cast(bf16x8, a), __builtin_bit_cast(bf16x8, b), c, 0, 0, 0); }
DEV unsigned canon(float v) { unsigned u = __float_as_uint(v); return u == SENT ? 0x7fc00000u : u; }
typedef f32x2 cf;
DEV cf pfma(cf a, cf b, cf c) { return __builtin_elementwise_fma(a, b, c); }
DEV cf cfma(cf a, cf b, cf c) { return pfma(cf{-a.y, a.y}, b.yx, pfma(a.xx, b, c)); }
DEV cf cmul(cf a, cf b) { return pfma(cf{-a.y, a.y}, b.yx, a.xx * b); }
DEV cf csel(bool p, cf a, cf b) { return {p ? a.x : b.x, p ? a.y : b.y}; }
DEV cf up2(unsigned u) { return {lo16(u), hi16(u)}; }
DEV unsigned pk2(cf v) { return __builtin_bit_cast(unsigned, __builtin_convertvector(v, bf16x2)); }
DEV cf sw16(float v) { return __builtin_bit_cast(cf, __builtin_amdgcn_permlane16_swap(__float_as_uint(v), __float_as_uint(v), false, false)); }
DEV cf sw32(float v) { return __builtin_bit_cast(cf, __builtin_amdgcn_permlane32_swap(__float_as_uint(v), __float_as_uint(v), false, false)); }
DEV float red16(float v) { cf t = sw16(v); return t.x + t.y; }
DEV float red32(float v) { cf t = sw32(v); return t.x + t.y; }
#define PIN(x) asm volatile("" : "+v"(x))
#define FENCE() do { asm volatile("" ::: "memory"); __builtin_amdgcn_sched_barrier(0); } while (0)
DEV rsrc_t mkrs(const void* p, unsigned n) { return __builtin_amdgcn_make_buffer_rsrc((void*)p, 0, n, 0x00020000); }
DEV u32x4 in16(rsrc_t rs, unsigned v, unsigned s) { return __builtin_amdgcn_raw_buffer_load_b128(rs, v, s, 0); }
DEV unsigned in4(rsrc_t rs, unsigned v) { return __builtin_amdgcn_raw_buffer_load_b32(rs, v, 0, 0); }
DEV u32x4 ld16(rsrc_t rs, unsigned v, unsigned s) { return __builtin_amdgcn_raw_buffer_load_b128(rs, v, s, 16); }
DEV u32x2 ld8(rsrc_t rs, unsigned v, unsigned s) { return __builtin_amdgcn_raw_buffer_load_b64(rs, v, s, 16); }
DEV unsigned ld4(rsrc_t rs, unsigned v, unsigned s) { return __builtin_amdgcn_raw_buffer_load_b32(rs, v, s, 16); }
DEV void st16(u32x4 d, rsrc_t rs, unsigned v, unsigned s) { __builtin_amdgcn_raw_buffer_store_b128(d, rs, v, s, 16); }
DEV void st8(u32x2 d, rsrc_t rs, unsigned v, unsigned s) { __builtin_amdgcn_raw_buffer_store_b64(d, rs, v, s, 16); }
DEV void st4(unsigned d, rsrc_t rs, unsigned v, unsigned s) { __builtin_amdgcn_raw_buffer_store_b32(d, rs, v, s, 16); }
DEV int wrap9(int v) { return v >= 9 ? v - 9 : v; }
DEV int gof(int g, int rot, int i) { return i == 0 ? g : wrap9(g + 1 + ((i - 1 + rot) & 7)); }
DEV int fsw(int row) { return ((row / 20) << 1) | ((row >> 1) & 1); }

DEV void poll_flags(rsrc_t rs, unsigned off, unsigned vo, u32x2 tag) {
#pragma unroll 1
  for (int it = 0; it < SPIN; ++it) {
    u32x2 v = ld8(rs, vo, off);
    if (__all(v.x == tag.x && v.y == tag.y)) break;
    asm volatile("s_sleep 1" ::: "memory");
  }
  asm volatile("" ::: "memory");
}

DEV void build_k(char* lds, int tid, u32x4 lo, u32x4 hi) {
  unsigned w[8] = {lo.x, lo.y, lo.z, lo.w, hi.x, hi.y, hi.z, hi.w}, o[7];
#pragma unroll
  for (int k = 0; k < 7; ++k) o[k] = __builtin_amdgcn_alignbit(w[k + 1], w[k], 16);
  char* p = lds + L_K + (tid >> 8) * 33280 + ((tid & 255) + 1) * 16;
#pragma unroll
  for (int s = 0; s < 8; ++s) {
    u32x4 v;
    if (s & 1) v = u32x4{w[(s + 1) / 2], w[(s + 1) / 2 + 1], w[(s + 1) / 2 + 2], w[(s + 1) / 2 + 3]};
    else v = u32x4{o[s / 2], o[s / 2 + 1], o[s / 2 + 2], o[s / 2 + 3]};
    *(u32x4*)(p + s * 4160) = v;
  }
  if (tid < 16) *(u32x4*)(lds + L_K + (tid >> 3) * 33280 + (tid & 7) * 4160) = u32x4{0, 0, 0, 0};
}
DEV void load_k(const u16* ssk, int d0, int tid, u32x4& lo, u32x4& hi) {
  const u16* kr = ssk + (size_t)(d0 + (tid >> 8)) * SEQ;
  int ci = tid & 255;
  hi = *(const u32x4*)(kr + 8 * ci);
  lo = *(const u32x4*)(kr + 8 * (ci > 0 ? ci - 1 : 0));
}
template <int SET, class FN> DEV void conv_mfma(const char* kb, const char* ub, f32x4 (&Y)[4], FN side) {
  constexpr int C0 = SET ? 1 : 0, C1 = SET ? 2 : 3, C2 = SET ? 4 : 5, C3 = SET ? 7 : 6, N = C3 + 1;
  u32x4 U[N], K[3];
#pragma unroll
  for (int p = C3; p >= 0; --p) U[p] = *(const u32x4*)(ub + 512 * (7 - p));
#pragma unroll
  for (int a = 0; a < 3; ++a) K[a] = *(const u32x4*)(kb + 512 * a);
  __builtin_amdgcn_sched_barrier(0);
#pragma unroll
  for (int st = 0; st < 4 * N; ++st) {
    const int rr = st / N, a = st % N, nx = st + 3;
    Y[3] = mfma(K[st % 3], U[C3 - a], Y[3]);
    if (C2 >= a) Y[2] = mfma(K[st % 3], U[C2 >= a ? C2 - a : 0], Y[2]);
    if (C1 >= a) Y[1] = mfma(K[st % 3], U[C1 >= a ? C1 - a : 0], Y[1]);
    if (C0 >= a) Y[0] = mfma(K[st % 3], U[C0 >= a ? C0 - a : 0], Y[0]);
    if (nx < 4 * N) K[st % 3] = *(const u32x4*)(kb + 512 * (nx % N) + 64 * (nx / N));
    if (rr < 3) U[C3 - a] = *(const u32x4*)(ub + 512 * (7 - (C3 - a)) + 64 * (rr + 1));
    side(st);
    __builtin_amdgcn_sched_barrier(0);
  }
}
template <class FN> DEV void conv_pair(char* lds, rsrc_t rs, int d0, int tid, int w, u32x4 rv, float dd, FN mid) {
  int l = tid & 63, chn = w >> 2, set = w & 1, rh = (w >> 1) & 1;
  if ((tid & 255) < 32) *(u32x4*)(lds + L_R + chn * 4608 + 4096 + l * 16) = u32x4{0, 0, 0, 0};
  *(u32x4*)(lds + L_R + chn * 4608 + (tid & 255) * 16) = rv;
  BAR();
  STAMP(20);
  int b = l & 15, g = l >> 4;
  const char* kb = lds + L_K + chn * 33280 + (b & 7) * 4160 + (g + (b >> 3)) * 16 + rh * 256;
  const char* ub = lds + L_R + chn * 4608 + (254 - 2 * b + g) * 16 - 3584 + rh * 256;
  f32x4 Y[4] = {};
  if (set) conv_mfma<1>(kb, ub, Y, mid); else conv_mfma<0>(kb, ub, Y, mid);
  STAMP(21);
  mid(-1);
  STAMP(22);
  f32x4 keep[2];
#pragma unroll
  for (int k = 0; k < 2; ++k) {
    f32x4 snd;
#pragma unroll
    for (int m = 0; m < 4; ++m) {
      float a = Y[k][m], bb = Y[2 + k][m];
      asm volatile("" : "+v"(a), "+v"(bb));
      snd[m] = rh ? a : bb; keep[k][m] = rh ? bb : a;
    }
    *(f32x4*)(lds + L_X + w * 2048 + k * 1024 + l * 16) = snd;
  }
  BAR();
  STAMP(23);
  u32x2 og[2];
#pragma unroll
  for (int k = 0; k < 2; ++k) {
    f32x4 y = keep[k] + *(const f32x4*)(lds + L_X + (w ^ 2) * 2048 + k * 1024 + l * 16);
    int cb = ((set ? 0x7421 : 0x6530) >> (4 * (2 * rh + k))) & 15;
    int tb = 256 * cb + 16 * b + 4 * g;
    u32x2 kv = *(const u32x2*)(lds + L_R + chn * 4608 + (2044 - tb) * 2);
    unsigned y01 = pk(y.x, y.y), y23 = pk(y.z, y.w);
    unsigned s01 = pk(hi16(kv.y) * dd, lo16(kv.y) * dd), s23 = pk(hi16(kv.x) * dd, lo16(kv.x) * dd);
    og[k] = u32x2{pk(lo16(y01) + lo16(s01), hi16(y01) + hi16(s01)), pk(lo16(y23) + lo16(s23), hi16(y23) + hi16(s23))};
  }
  u32x2 sx = __builtin_amdgcn_permlane16_swap(og[0].x, og[1].x, false, false), sy = __builtin_amdgcn_permlane16_swap(og[0].y, og[1].y, false, false);
  int cb = ((set ? 0x7421 : 0x6530) >> (4 * (2 * rh + (g & 1)))) & 15;
  st16(u32x4{sx.x, sy.x, sx.y, sy.y}, rs, 512 * cb + 32 * b + 16 * (g >> 1), WS_GT + (d0 + chn) * 4096);
}
constexpr int RG = 4;
template <int KIND> DEV u32x4 frag(const char* lds, int fb, int s) {
  if (KIND == 1) return *(const u32x4*)(lds + (fb ^ (((s / 5) & 1) * 64)) + (s / 10) * 10240 + (s % 5) * 512);
  return *(const u32x4*)(lds + (fb ^ (((s / 5) & 1) * 16)) + ((s / 10 + 5) % 6 + (s < 10 ? -1 : 0)) * 20480 + (s % 5) * 4096);
}
template <int KIND, int S0, int S1, class FN>
DEV void gsteps(const char* lds, int fb, const u32x4 (&W)[18], f32x4 (&acc)[5], u32x4 (&F)[RG + 1], FN side) {
#pragma unroll
  for (int s = S0; s < S1; ++s) {
    const int m = s - RG;
    if (s < 90) F[s % (RG + 1)] = frag<KIND>(lds, fb, s);
    __builtin_amdgcn_sched_barrier(0);
    if (m >= 0) acc[m % 5] = KIND == 1 ? mfma(F[m % (RG + 1)], W[m / 5], acc[m % 5]) : mfma(W[m / 5], F[m % (RG + 1)], acc[m % 5]);
    side(s);
    __builtin_amdgcn_sched_barrier(0);
  }
}
DEV void ldb(rsrc_t rs, u32x4* T, unsigned so, int l) {
#pragma unroll
  for (int n = 0; n < 5; ++n) T[n] = ld16(rs, l * 16 + n * 1024, so);
}
DEV void need(rsrc_t rs, u32x4* T, unsigned so, int l) {
#pragma unroll 1
  for (int it = 0; it < SPIN; ++it) {
    unsigned m = 0;
#pragma unroll
    for (int n = 0; n < 5; ++n) m = max(max(max(m, T[n].x), T[n].y), max(T[n].z, T[n].w));
    if (!__any(m == SENT)) break;
    asm volatile("" ::: "memory");
    ldb(rs, T, so, l);
  }
}
#define PINS(x) asm volatile("" : "+s"(x))

__global__ __launch_bounds__(512, 2) void fused_kernel(
    const u16* __restrict__ X, const u16* __restrict__ Lre, const u16* __restrict__ Lim, const u16* __restrict__ Bre,
    const u16* __restrict__ Bim, const u16* __restrict__ Cre, const u16* __restrict__ Cim, const u16* __restrict__ logdt,
    const u16* __restrict__ Dsk, const u16* __restrict__ Kin, const u16* __restrict__ Vin, const u16* __restrict__ shk,
    const u16* __restrict__ ssk, const u16* __restrict__ sD, float* __restrict__ out, char* ws) {
  __shared__ __attribute__((aligned(16))) char lds[163840];
  int tid = threadIdx.x, l = tid & 63;
  const int w = __builtin_amdgcn_readfirstlane(tid >> 6);
  const int xcd = blockIdx.x & 7, loc = blockIdx.x >> 3;
  int c, g, spare = -1;
  if (loc < 27) { c = 3 * xcd + loc / 9; g = loc % 9; }
  else {
    int idx = xcd * 5 + loc - 27;
    if (idx >= 36) return;
    if (idx < 18) { c = 24 + idx / 9; g = idx % 9; } else { spare = idx - 18; c = 0; g = spare >> 1; }
  }
  asm volatile("" : "+s"(ws));
  const rsrc_t rs = __builtin_amdgcn_make_buffer_rsrc(ws, 0, WS_END, 0x00020000);
  for (int i = 30; i >= 0; --i) STAMP(i);
#define GETTAG() do { tag.x = __builtin_amdgcn_readfirstlane(tag.x) + 1; tag.y = __builtin_amdgcn_readfirstlane(tag.y) + 1; } while (0)
  if (spare >= 0) {
    u32x2 tag = ld8(rs, 0, 0);
    const int d0 = 64 * g + 2 * (26 + 3 * (spare & 1));
    u32x4 klo[3], khi[3], rv[3];
    {
      const rsrc_t r0 = mkrs(Cre, DM * RB), r1 = mkrs(Cim, DM * RB);
      const unsigned dro = (64 * g + 16 * (w & 3) + (l & 15)) * RB + 64 * (w >> 2) + 16 * (l >> 4);
      const int i0 = 4 * (spare & 1);
      u32x4 re[5], im[5];
#pragma unroll
      for (int i = 0; i < 5; ++i) { re[i] = in16(r0, dro, (i0 + i) * 128); im[i] = in16(r1, dro, (i0 + i) * 128); }
#pragma unroll
      for (int i = 0; i < 5; ++i)
#pragma unroll
        for (int k = 0; k < 2; ++k) {
          unsigned a = re[i][2 * k], b = im[i][2 * k] ^ 0x80008000u, a2 = re[i][2 * k + 1], b2 = im[i][2 * k + 1] ^ 0x80008000u;
          u32x4 o = {__builtin_amdgcn_perm(b, a, 0x05040100), __builtin_amdgcn_perm(b, a, 0x07060302),
                     __builtin_amdgcn_perm(b2, a2, 0x05040100), __builtin_amdgcn_perm(b2, a2, 0x07060302)};
          if (i > 0 || i0 == 0) st16(o, rs, l * 16 + k * 1024, WS_W2 + ((g * 9 + i0 + i) * 8 + w) * 2048);
        }
    }
    GETTAG();
    DRAIN();
    BAR();
    if (tid == 0) st8(tag, rs, 0, WS_KVF + g * 256 + (NP + (spare & 1)) * 8);
#pragma unroll
    for (int r = 0; r < 3; ++r) load_k(ssk, d0 + 2 * r, tid, klo[r], khi[r]);
#pragma unroll 1
    for (int r = 0; r < 3; ++r) {
      if ((tid & 255) == 0) klo[0] = u32x4{0, 0, 0, 0};
      build_k(lds, tid, klo[0], khi[0]);
      if (r == 0) {
        poll_flags(rs, WS_KVF + g * 256, min(l, NP - 1) * 8, tag);
#pragma unroll
        for (int k = 0; k < 3; ++k) rv[k] = ld16(rs, (tid & 255) * 16, WS_RKV + (d0 + 2 * k + (w >> 2)) * 4096);
      }
      conv_pair(lds, rs, d0 + 2 * r, tid, w, rv[0], b2f(sD[d0 + 2 * r + (w >> 2)]), [](int) {});
      klo[0] = klo[1]; klo[1] = klo[2]; khi[0] = khi[1]; khi[1] = khi[2]; rv[0] = rv[1]; rv[1] = rv[2];
      BAR();
    }
    DRAIN();
    BAR();
    if (tid == 0) st8(tag, rs, 0, WS_GF + g * 256 + (26 + (spare & 1)) * 8);
    STAMP(1);
    DUMP();
    return;
  }
  asm volatile("" ::: "memory");
  u32x2 tag = ld8(rs, 0, 0);
  const int t0 = TC * c, me = c * 9 + g, rot = 3 * (c % 3);
  int q = l >> 4, ri = l & 1, pp = (l & 15) >> 1, s8 = 2 * q + ri, er, ei, eo0;
#define REDERIVE() do { asm volatile("" : "+v"(tid)); l = tid & 63; q = l >> 4; ri = l & 1; pp = (l & 15) >> 1; s8 = 2 * q + ri; \
    er = tid >> 2; ei = l & 3; eo0 = er * 128 + ((ei ^ fsw(er)) << 4); } while (0)
  const u32x4 S4 = {SENT, SENT, SENT, SENT};
  auto clr = [&](unsigned so, int n) {
#pragma unroll
    for (int k = 0; k < n; k += 512) st16(S4, rs, k + 512 <= n || tid < n - k ? tid * 16 : OOB, so + k * 16);
  };
  const rsrc_t rx = mkrs(X, SEQ * RB);
  unsigned kk[11], vv[8]; u32x4 shv;
  const int dp = l & 31, tb = t0 + 8 * (2 * w + (l >> 5));
  if (w < 5) {
    const rsrc_t rk = mkrs(Kin, SEQ * RB), rvv = mkrs(Vin, SEQ * RB);
    unsigned b1 = tb * RB + 128 * g + 4 * dp, b0 = tb ? b1 - 3 * RB : OOB, b2 = b1 + 4 * RB;
    PIN(b0); PIN(b1); PIN(b2);
    shv = *(const u32x4*)(shk + (64 * g + 2 * dp) * 4);
#pragma unroll
    for (int j = 0; j < 11; ++j) kk[j] = in4(rk, j < 3 ? b0 + j * RB : j < 7 ? b1 + (j - 3) * RB : b2 + (j - 7) * RB);
#pragma unroll
    for (int j = 0; j < 8; ++j) vv[j] = in4(rvv, j < 4 ? b1 + j * RB : b2 + (j - 4) * RB);
  }
  FENCE();
  const int p = 64 * g + 8 * w + pp;
  u32x4 W1[18];
  auto ldw1 = [&](int i0) {
    const rsrc_t r0 = mkrs(Bre, DM * RB), r1 = mkrs(Bim, DM * RB);
    const unsigned wo = (64 * g + 8 * w + (l >> 3)) * RB + (l & 7) * 16;
#pragma unroll
    for (int i = i0; i < 9; ++i) { W1[2 * i] = in16(r0, wo, gof(g, rot, i) * 128); W1[2 * i + 1] = in16(r1, wo, gof(g, rot, i) * 128); }
  };
  auto fixw1 = [&](int i0, int base) {
    const int sw = base + (l >> 3) * 128 + (((l & 7) ^ ((l >> 4) * 2)) << 4);
    const int sr = base + ri * 1024 + pp * 128 + ((q ^ ((pp >> 1) * 2 + ri)) << 4);
#pragma unroll
    for (int i = i0; i < 9; ++i) {
      *(u32x4*)(lds + sw + (i & 3) * 2048) = W1[2 * i];
      *(u32x4*)(lds + (sw ^ 16) + (i & 3) * 2048 + 1024) = W1[2 * i + 1];
      FENCE();
      W1[2 * i] = *(const u32x4*)(lds + sr + (i & 3) * 2048);
      W1[2 * i + 1] = *(const u32x4*)(lds + (sr ^ 64) + (i & 3) * 2048);
      FENCE();
    }
  };
  ldw1(0);
  FENCE();
  u32x4 xa[9], xb[3];
  const int row = tid >> 3, row2 = 64 + (tid >> 5), e = tid & 31;
  {
    unsigned xo = (t0 + row) * RB + (tid & 7) * 16, xo2 = (t0 + row2) * RB + (e & 7) * 16;
#pragma unroll
    for (int i = 0; i < 9; ++i) xa[i] = in16(rx, xo, gof(g, rot, i) * 128);
    xb[0] = in16(rx, xo2 + gof(g, rot, e >> 3) * 128, 0);
    xb[1] = in16(rx, xo2 + gof(g, rot, (e >> 3) + 4) * 128, 0);
    xb[2] = in16(rx, e < 8 ? xo2 : OOB, gof(g, rot, 8) * 128);
  }
  u32x4 klo, khi;
  load_k(ssk, 64 * g + 2 * c, tid, klo, khi);
  const u16 lre_ = Lre[p], lim_ = Lim[p], ldt_ = logdt[p];
  clr(WS_BLK + me * BLK, BLK_CH);
  clr(WS_HT + me * 20480, 1280);
  FENCE();
  if (w < 5) {
    cf sh[4] = {{lo16(shv.x), lo16(shv.z)}, {hi16(shv.x), hi16(shv.z)}, {lo16(shv.y), lo16(shv.w)}, {hi16(shv.y), hi16(shv.w)}};
    cf P[8];
#pragma unroll
    for (int j = 0; j < 8; ++j) {
      cf a = up2(kk[j]) * sh[3];
#pragma unroll
      for (int tt = 1; tt < 4; ++tt) a = pfma(up2(kk[j + tt]), sh[3 - tt], a);
      P[j] = a * up2(vv[j]);
    }
    u32x4 o0, o1;
#pragma unroll
    for (int k = 0; k < 4; ++k) { o0[k] = pk(P[7 - 2 * k].x, P[6 - 2 * k].x); o1[k] = pk(P[7 - 2 * k].y, P[6 - 2 * k].y); }
    unsigned vo = tb < SEQ ? 2 * dp * 4096 + (2040 - tb) * 2 : OOB;
    st16(o0, rs, vo, WS_RKV + 64 * g * 4096);
    st16(o1, rs, vo, WS_RKV + (64 * g + 1) * 4096);
  }
  STAMP(17);
  FENCE();
  float cs;
  {
    fixw1(0, L_K + w * 8192);
    f32x4 ca = {};
#pragma unroll
    for (int i = 0; i < 18; ++i) ca = mfma(u32x4{0x3f803f80u, 0x3f803f80u, 0x3f803f80u, 0x3f803f80u}, W1[i], ca);
    cs = ca.x;
    PIN(cs);
  }
  FENCE();
  {
    float s1 = 0.f, s2 = 0.f, u1 = 0.f, u2 = 0.f;
    char* d = lds + row * 128 + (((tid & 7) ^ fsw(row)) << 4);
#pragma unroll
    for (int i = 0; i < 9; ++i) {
      *(u32x4*)(d + i * 10240) = xa[i];
#pragma unroll
      for (int k = 0; k < 4; ++k) { s1 = dot2(xa[i][k], 0x3f803f80u, s1); s2 = dot2(xa[i][k], xa[i][k], s2); }
    }
    char* d2 = lds + row2 * 128 + (((e & 7) ^ fsw(row2)) << 4) + (e >> 3) * 10240;
#pragma unroll
    for (int n = 0; n < 3; ++n) {
      if (n < 2 || e < 8) *(u32x4*)(d2 + n * 40960) = xb[n];
#pragma unroll
      for (int k = 0; k < 4; ++k) { u1 = dot2(xb[n][k], 0x3f803f80u, u1); u2 = dot2(xb[n][k], xb[n][k], u2); }
    }
    s1 += dpp<0xB1>(s1); s2 += dpp<0xB1>(s2); u1 += dpp<0xB1>(u1); u2 += dpp<0xB1>(u2);
    s1 += dpp<0x4E>(s1); s2 += dpp<0x4E>(s2); u1 += dpp<0x4E>(u1); u2 += dpp<0x4E>(u2);
    s1 += dpp<0x141>(s1); s2 += dpp<0x141>(s2); u1 += dpp<0x141>(u1); u2 += dpp<0x141>(u2);
    u1 += dpp<0x140>(u1); u2 += dpp<0x140>(u2);
    u1 = red16(u1); u2 = red16(u2);
    bool ex = (tid & 31) == 1;
    float a1 = (ex ? u1 : s1) * (1.f / DM), a2 = (ex ? u2 : s2) * (1.f / DM);
    float rsd = __builtin_amdgcn_rcpf(rb(__builtin_amdgcn_sqrtf(rb(rb(fmaxf(a2 - a1 * a1, 0.f)) + 1e-5f))));
    if (ex || (tid & 7) == 0) *(f32x2*)(lds + L_M + (ex ? row2 : row) * 8) = f32x2{rsd, -rb(a1)};
  }
  STAMP(18);
  GETTAG();
  float lre = b2f(lre_), lim = b2f(lim_), ldt = b2f(ldt_);
  cf lam, co;
  {
    float dt = rb(__expf(ldt)), ee = __expf(dt * lre), rev = __builtin_amdgcn_fractf(dt * lim * 0.15915494309189535f);
    lam = cf{__builtin_amdgcn_cosf(rev), __builtin_amdgcn_sinf(rev)} * ee;
    float den = 2.f * __builtin_amdgcn_rcpf(lre * lre + lim * lim), nr = lam.x - 1.f, ni = lam.y;
    co = {(nr * lre + ni * lim) * den, (ni * lre - nr * lim) * den};
    *(f32x4*)(lds + L_M + 1024 + (8 * w + pp) * 16) = f32x4{lam.x, lam.y, co.x, co.y};
  }
  DRAIN();
  BAR();
  if (tid == 0) st8(tag, rs, 0, WS_KVF + g * 256 + c * 8);
  STAMP(1);
  if ((tid & 255) == 0) klo = u32x4{0, 0, 0, 0};
  build_k(lds, tid, klo, khi);
  STAMP(19);
  u32x4 W2[18];
  constexpr int W2A = 5, DLY_X = 8, DLY_H0 = 8, DLY_H1 = 12;
  auto ldw2 = [&](int i0, int i1) {
#pragma unroll
    for (int i = i0; i < i1; ++i) {
      const unsigned so = WS_W2 + ((g * 9 + gof(g, rot, i)) * 8 + w) * 2048;
      W2[2 * i] = ld16(rs, l * 16, so); W2[2 * i + 1] = ld16(rs, l * 16 + 1024, so);
    }
  };

#pragma unroll
  for (int L = 0; L < 2; ++L) {
    f32x4 acc[5] = {};
    u32x4 F[RG + 1];
    REDERIVE();
    const int ab = (20 * ((l & 15) >> 2) + (l & 3)) * 128 + ((q ^ ((l & 15) >> 1)) << 4);
    cf l10, l20;
    if (L) fixw1(8, 102400 + w * 2048);
    if (L) { f32x4 t = *(const f32x4*)(lds + L_M + 1024 + (8 * w + pp) * 16); lam = t.xy; co = t.zw; }
    cf cA = csel(ri, cf{-co.y, co.x}, co), cB = csel(ri, co, cf{-co.y, co.x});
    PIN(cA); PIN(cB);
    {
      cf l2 = cmul(lam, lam), l4 = cmul(l2, l2), l5 = cmul(l4, lam);
      l10 = cmul(l5, l5); l20 = cmul(l10, l10);
    }
    PIN(l10); PIN(l20);
    cf CS;
    u32x4 rv;
    if (L == 0) {
      u32x2 fl;
      unsigned fa = g * 256, fb = c * 8;
      PINS(fa); PINS(fb);
      const unsigned fo = l < NP + 2 ? fa + l * 8 : min(l - NP - 2, 8) * 256 + fb;
      gsteps<1, 0, 90 + RG>(lds, ab, W1, acc, F, [&](int s) {
        if (s == 60) fl = ld8(rs, fo, WS_KVF);
      });
      STAMP(2);
      if (!__all(fl.x == tag.x && fl.y == tag.y)) poll_flags(rs, WS_KVF, fo, tag);
      STAMP(3);
      rv = ld16(rs, (tid & 255) * 16, WS_RKV + (64 * g + 2 * c + (w >> 2)) * 4096);
      FENCE();
      CS = pfma(cA, cf{cs, cs}, cB * dpp<0xB1>(cs));
    } else {
      u32x4 T[10];
      unsigned sA = WS_XT + (c * 9 + gof(g, rot, 1 + (w >> 1))) * 10240 + (w & 1) * 5120;
      unsigned sB = WS_XT + (c * 9 + gof(g, rot, 5 + (w >> 1))) * 10240 + (w & 1) * 5120;
      char* td = lds + w * 5120 + l * 16;
      gsteps<1, 0, 10>(lds, ab, W1, acc, F, [&](int s) {});
      asm volatile("s_sleep %0" :: "n"(DLY_X) : "memory");
      ldb(rs, T, sA, l); ldb(rs, T + 5, sB, l);
      need(rs, T, sA, l);
      STAMP(10);
#pragma unroll
      for (int n = 0; n < 5; ++n) *(u32x4*)(td + 10240 + n * 1024) = T[n];
      BAR();
      gsteps<1, 10, 50>(lds, ab, W1, acc, F, [&](int s) {
        if (s == 16) need(rs, T + 5, sB, l);
        if (s % 4 == 0 && s >= 16 && s < 36) *(u32x4*)(td + 51200 + (s / 4 - 4) * 1024) = T[5 + (s / 4 - 4) % 5];
      });
      BAR();
      gsteps<1, 50, 90 + RG>(lds, ab, W1, acc, F, [&](int s) {});
      STAMP(11);
    }
    REDERIVE();
    cf u[10], hin;
    {
#pragma unroll
      for (int j = 0; j < 10; ++j) {
        float lo = acc[j >> 2][j & 3], hi = acc[(10 + j) >> 2][(10 + j) & 3];
        float kept = ri ? hi : lo, rcv = dpp<0xB1>(ri ? lo : hi);
        if (L == 0) {
          cf ms = *(const cf*)(lds + L_M + (10 * s8 + j) * 8);
          u[j] = pfma(cA, cf{kept, kept}, pfma(cB, cf{rcv, rcv}, CS * ms.y)) * ms.x;
        } else {
          u[j] = pfma(cA, cf{kept, kept}, cB * rcv);
        }
      }
      cf h = u[0];
#pragma unroll
      for (int j = 1; j < 10; ++j) h = cfma(lam, h, u[j]);
      cf o = {dpp<0xB1>(h.x), dpp<0xB1>(h.y)};
      cf ev = csel(ri, o, h), od = csel(ri, h, o);
      cf P = cfma(l10, ev, od);
      cf ax = sw16(P.x), ay = sw16(P.y);
      cf Pe = {ax.x, ay.x}, Po = {ax.y, ay.y}, D = cfma(l20, Pe, Po);
      cf bx = sw32(D.x), by = sw32(D.y), S2 = {bx.x, by.x}, l40 = cmul(l20, l20);
      cf E = cfma(l40, S2, cf{bx.y, by.y});
      if (s8 == 0) st8(u32x2{canon(E.x), canon(E.y)}, rs, (8 * w + pp) * 8, WS_BLK + me * BLK + L * 512);
      cf Sb = csel(q & 2, S2, cf{0.f, 0.f}), Sq = csel(q & 1, cfma(l20, Sb, Pe), Sb);
      hin = csel(ri, cfma(l10, Sq, ev), Sq);
    }
    u32x2 ev[4];
    const unsigned evs = WS_BLK + g * BLK + L * 512;
    auto ldev = [&] {
      const unsigned evo = (c - 1 - s8) * (9 * BLK) + (8 * w + pp) * 8;
#pragma unroll
      for (int i = 0; i < 4; ++i) ev[i] = ld8(rs, evo - i * (72 * BLK), evs);
    };
    BAR();
    if (L == 0) {
      STAMP(26);
      conv_pair(lds, rs, 64 * g + 2 * c, tid, w, rv, b2f(sD[64 * g + 2 * c + (w >> 2)]), [&](int st) {
        if (st < 0) { REDERIVE(); ldev(); ldw2(W2A, 9); FENCE(); }
        else if (st % 4 == 1 && st / 4 < W2A) ldw2(st / 4, st / 4 + 1);
      });
      STAMP(4);
    } else {
      ldev();
    }
    REDERIVE();
    {
      cf one = {1.f, 0.f}, l40 = cmul(l20, l20), l80 = cmul(l40, l40), l160 = cmul(l80, l80), l320 = cmul(l160, l160);
      cf l640 = cmul(l320, l320);
      cf l10s = cmul(cmul(csel(ri, l10, one), csel(q & 1, l20, one)), csel(q & 2, l40, one));
      cf l80s = cmul(cmul(csel(ri, l80, one), csel(q & 1, l160, one)), csel(q & 2, l320, one));
      int ha[10];
#pragma unroll
      for (int j = 0; j < 10; ++j) {
        int t = 10 * s8 + j;
        ha[j] = L_H + t * 256 + (((2 * w + (pp >> 2)) ^ (t & 15)) << 4) + (pp & 3) * 4;
        PIN(ha[j]);
      }
      PIN(l640); PIN(l10s); PIN(l80s);
#pragma unroll 1
      for (int it = 0; it < SPIN; ++it) {
        bool ok = true;
#pragma unroll
        for (int i = 0; i < 4; ++i) ok = ok && ev[i].x != SENT && ev[i].y != SENT;
        if (__all(ok)) break;
        asm volatile("" ::: "memory");
        ldev();
      }
      cf H = __builtin_bit_cast(cf, ev[3]);
#pragma unroll
      for (int i = 2; i >= 0; --i) H = cfma(l640, H, __builtin_bit_cast(cf, ev[i]));
      H = cmul(l80s, H);
      H.x += dpp<0xB1>(H.x); H.y += dpp<0xB1>(H.y);
      H = {red32(red16(H.x)), red32(red16(H.y))};
      hin = cfma(l10s, H, hin);
      STAMP(5 + 7 * L);
#pragma unroll
      for (int j = 0; j < 10; ++j) {
        hin = cfma(lam, hin, u[j]);
        *(unsigned*)(lds + ha[j]) = pk2(hin);
      }
    }
    BAR();
    const unsigned hbase = WS_HT + (L * NP + c) * 9 * 20480;
#pragma unroll
    for (int n = 0; n < 3; ++n)
      if (n < 2 || w < 4) st16(*(const u32x4*)(lds + L_H + tid * 16 + n * 8192), rs, tid * 16 + n * 8192, hbase + g * 20480);
    STAMP(6 + 7 * L);
    if (L == 0) {
      u32x4 xs[4];
      xs[0] = in16(rx, (t0 + er) * RB + 16 * ei, 128 * g); xs[1] = in16(rx, (t0 + er) * RB + 16 * ei + 64, 128 * g);
      xs[2] = *(const u32x4*)(Dsk + 64 * g + 8 * ei); xs[3] = *(const u32x4*)(Dsk + 64 * g + 32 + 8 * ei);
      FENCE();
      if (w < 5) {
        cf ms = *(const cf*)(lds + L_M + er * 8);
#pragma unroll
        for (int k = 0; k < 4; ++k) {
          cf x0 = up2(xs[k >> 1][2 * (k & 1)]) + ms.y, x1 = up2(xs[k >> 1][2 * (k & 1) + 1]) + ms.y;
          x0 = pfma(x0 * ms.x, up2(xs[2 + (k >> 1)][2 * (k & 1)]), x0);
          x1 = pfma(x1 * ms.x, up2(xs[2 + (k >> 1)][2 * (k & 1) + 1]), x1);
          *(f32x4*)(lds + L_B + k * 5120 + tid * 16) = f32x4{x0.x, x0.y, x1.x, x1.y};
        }
      }
    }
    cf G[8], z[8];
    u32x4 xs[4];
    {
      f32x4 y[5] = {};
      REDERIVE();
      const int hb = (l & 15) * 256 + (((8 * (w >> 2) + 2 * q) ^ (l & 15)) << 4);
      u32x4 T[10], gv[2];
      u32x2 fl;
      unsigned so[4];
#pragma unroll
      for (int k = 0; k < 4; ++k) so[k] = hbase + gof(g, rot, 1 + 2 * k + (w >> 2)) * 20480 + (w & 3) * 5120;
      char* td = lds + w * 5120 + l * 16;
      gsteps<2, 0, 10>(lds, hb, W2, y, F, [&](int s) {});
      asm volatile("s_sleep %0" :: "n"(L ? DLY_H1 : DLY_H0) : "memory");
      ldb(rs, T, so[0], l); ldb(rs, T + 5, so[1], l);
      if (L == 0) { FENCE(); clr(WS_XT + me * 10240, 640); clr(WS_HT + (234 + me) * 20480, 1280); }
      need(rs, T, so[0], l);
      STAMP(7 + 7 * L);
#pragma unroll
      for (int n = 0; n < 5; ++n) { *(u32x4*)(td + n * 1024) = T[n]; T[n] = ld16(rs, l * 16 + n * 1024, so[2]); }
      BAR();
      if (L == 0 && tid == 0) st8(tag, rs, 0, WS_GF + g * 256 + c * 8);
      gsteps<2, 10, 30>(lds, hb, W2, y, F, [&](int s) {
        if (s == 12) need(rs, T + 5, so[1], l);
        if (s % 3 == 0 && s < 27) { const int n = (s / 3 - 4) % 5; *(u32x4*)(td + 40960 + n * 1024) = T[5 + n]; T[5 + n] = ld16(rs, l * 16 + n * 1024, so[3]); }
      });
      BAR();
      gsteps<2, 30, 50>(lds, hb, W2, y, F, [&](int s) {
        if (s == 33) need(rs, T, so[2], l);
        if (s % 3 == 0 && s >= 33 && s < 48) { const int n = (s / 3 - 11) % 5; *(u32x4*)(td + 81920 + n * 1024) = T[n]; }
        if (L == 0 && s == 34) fl = ld8(rs, min(l, 27) * 8, WS_GF + g * 256);
      });
      if (L == 0) {
        if (!__all(fl.x == tag.x && fl.y == tag.y)) poll_flags(rs, WS_GF + g * 256, min(l, 27) * 8, tag);
#pragma unroll
        for (int n = 0; n < 2; ++n) {
          int id = min(tid + 512 * n, 639), dl = id / 10, ck = id - 10 * dl;
          gv[n] = ld16(rs, dl * 4096 + min(t0 + 8 * ck, SEQ - 8) * 2, WS_GT + 64 * g * 4096);
        }
      }
      BAR();
      gsteps<2, 50, 70>(lds, hb, W2, y, F, [&](int s) {
        if (s == 51) need(rs, T + 5, so[3], l);
        if (s % 3 == 0 && s < 66) { const int n = (s / 3 - 17) % 5; *(u32x4*)(td + n * 1024) = T[5 + n]; }
      });
      BAR();
      REDERIVE();
      if (L == 0) { xs[2] = *(const u32x4*)(Dsk + 64 * g + 8 * ei); xs[3] = *(const u32x4*)(Dsk + 64 * g + 32 + 8 * ei); }
      gsteps<2, 70, 90 + RG>(lds, hb, W2, y, F, [&](int s) {});
#pragma unroll
      for (int tt = 0; tt < 5; ++tt)
        *(f32x4*)(lds + L_P + (w >> 2) * 21760 + (16 * tt + (l & 15)) * 272 + (16 * (w & 3) + 4 * q) * 4) = y[tt];
      if (L == 0) {
#pragma unroll
        for (int n = 0; n < 2; ++n) {
          int id = min(tid + 512 * n, 639), dl = id / 10;
          *(u32x4*)(lds + L_G + id * 16 + ((dl >> 3) & 3) * 32 + (dl >> 5) * 128) = gv[n];
        }
        BAR();
      }
      {
        const char* gp = lds + L_G + 8 * ei * 160 + ei * 32 + er * 2;
#pragma unroll
        for (int k = 0; k < 4; ++k) { f32x4 b = *(const f32x4*)(lds + L_B + k * 5120 + tid * 16); z[2 * k] = b.xy; z[2 * k + 1] = b.zw; }
#pragma unroll
        for (int k = 0; k < 8; ++k) {
          int o = (k >> 2) * (32 * 160 + 128) + (k & 3) * 320;
          G[k] = {b2f(*(const u16*)(gp + o)), b2f(*(const u16*)(gp + o + 160))};
        }
      }
      if (L == 1) BAR();
    }
    STAMP(8 + 7 * L);
    if (L == 0) { ldw1(8); FENCE(); }
    if (w < 5) {
      const char* pb = lds + L_P + er * 272 + ei * 32;
      const float piv = *(const float*)(lds + L_M + er * 8 + 4 - 4 * L);
#pragma unroll
      for (int k = 0; k < 4; ++k) {
        int o = (k >> 1) * 128 + (k & 1) * 16;
        f32x4 a = *(const f32x4*)(pb + o), b = *(const f32x4*)(pb + 21760 + o);
        z[2 * k] += a.xy + b.xy; z[2 * k + 1] += a.zw + b.zw;
      }
      if (L == 0) {
        cf S[8] = {};
#pragma unroll
        for (int k = 0; k < 8; ++k) {
          cf zg = z[k] * G[k];
          S[0] += z[k]; S[1] = pfma(z[k], z[k], S[1]); S[2] += zg; S[3] = pfma(z[k], zg, S[3]);
          S[4] = pfma(zg, zg, S[4]); S[5] = pfma(zg, G[k], S[5]); S[6] += G[k]; S[7] = pfma(G[k], G[k], S[7]);
        }
        float s[8], r[4];
#pragma unroll
        for (int k = 0; k < 8; ++k) s[k] = S[k].x + S[k].y;
#pragma unroll
        for (int k = 0; k < 4; ++k) { const int a = (k >> 1) * 4 + (k & 1); r[k] = ((ei & 1) ? s[a + 2] : s[a]) + dpp<0xB1>((ei & 1) ? s[a] : s[a + 2]); }
        float v0 = ((ei & 2) ? r[2] : r[0]) + dpp<0x4E>((ei & 2) ? r[0] : r[2]);
        float v1 = ((ei & 2) ? r[3] : r[1]) + dpp<0x4E>((ei & 2) ? r[1] : r[3]);
        st8(u32x2{canon(v0), canon(v1)}, rs, tid * 8, WS_BLK + me * BLK + BLK_S1);
        STAMP(24);
        u32x2 sv[8];
#pragma unroll 1
        for (int it = 0; it < SPIN; ++it) {
          bool ok = true;
#pragma unroll
          for (int k = 0; k < 8; ++k) { sv[k] = ld8(rs, tid * 8, WS_BLK + (c * 9 + wrap9(g + 1 + k)) * BLK + BLK_S1); ok = ok && sv[k].x != SENT && sv[k].y != SENT; }
          if (__all(ok)) break;
          asm volatile("" ::: "memory");
        }
        STAMP(25);
        cf V = {v0, v1};
#pragma unroll
        for (int k = 0; k < 8; ++k) V += __builtin_bit_cast(cf, sv[k]);
        V *= 1.f / DM;
        float mu = dpp<0x00>(V.x), zz = dpp<0x00>(V.y), zG = dpp<0x55>(V.x), zzG = dpp<0x55>(V.y);
        float zzGG = dpp<0xAA>(V.x), zGG = dpp<0xAA>(V.y), G1 = dpp<0xFF>(V.x), G2 = dpp<0xFF>(V.y);
        float a = __builtin_amdgcn_rsqf(fmaxf(zz - mu * mu, 0.f) + 1e-5f);
        float mw = mu + a * (zG - mu * G1);
        float ww = zz + 2.f * a * (zzG - mu * zG) + a * a * (zzGG - 2.f * mu * zGG + mu * mu * G2);
        float a2 = __builtin_amdgcn_rsqf(fmaxf(ww - mw * mw, 0.f) + 1e-5f), am = -a * mu;
        u32x4 o[2];
#pragma unroll
        for (int k = 0; k < 8; ++k) {
          z[k] = pfma(pfma(z[k], cf{a, a}, cf{am, am}), G[k], z[k] - mw);
          G[k] = z[k] * a2;
          o[k >> 2][k & 3] = pk2(G[k]);
        }
        st16(o[0], rs, eo0, WS_XT + me * 10240);
        st16(o[1], rs, eo0 ^ 64, WS_XT + me * 10240);
        FENCE();
        *(float*)(lds + L_M + er * 8) = mw - piv;
        *(u32x4*)(lds + eo0) = o[0];
        *(u32x4*)(lds + (eo0 ^ 64)) = o[1];
#pragma unroll
        for (int k = 0; k < 4; ++k) {
          cf b0 = pfma(G[2 * k], up2(xs[2 + (k >> 1)][2 * (k & 1)]), z[2 * k]);
          cf b1 = pfma(G[2 * k + 1], up2(xs[2 + (k >> 1)][2 * (k & 1) + 1]), z[2 * k + 1]);
          *(f32x4*)(lds + L_B + k * 5120 + tid * 16) = f32x4{b0.x, b0.y, b1.x, b1.y};
        }
      } else {
        cf S0 = z[0], S1 = z[0] * z[0];
#pragma unroll
        for (int k = 1; k < 8; ++k) { S0 += z[k]; S1 = pfma(z[k], z[k], S1); }
        float s0 = S0.x + S0.y, s1 = S1.x + S1.y;
        s0 += dpp<0xB1>(s0); s1 += dpp<0xB1>(s1); s0 += dpp<0x4E>(s0); s1 += dpp<0x4E>(s1);
        if (ei == 0) st8(u32x2{canon(s0), canon(s1)}, rs, er * 8, WS_BLK + me * BLK + BLK_S2);
        u32x2 sv[2];
        const unsigned so[2] = {er * 8 + wrap9(g + 1 + ei) * BLK, er * 8 + wrap9(g + 5 + ei) * BLK};
#pragma unroll 1
        for (int it = 0; it < SPIN; ++it) {
          bool ok = true;
#pragma unroll
          for (int k = 0; k < 2; ++k) { sv[k] = ld8(rs, so[k], WS_BLK + c * 9 * BLK + BLK_S2); ok = ok && sv[k].x != SENT && sv[k].y != SENT; }
          if (__all(ok)) break;
          asm volatile("" ::: "memory");
        }
        cf V = __builtin_bit_cast(cf, sv[0]) + __builtin_bit_cast(cf, sv[1]);
        V.x += dpp<0xB1>(V.x); V.y += dpp<0xB1>(V.y); V.x += dpp<0x4E>(V.x); V.y += dpp<0x4E>(V.y);
        float mu = (s0 + V.x) * (1.f / DM), a = __builtin_amdgcn_rsqf(fmaxf((s1 + V.y) * (1.f / DM) - mu * mu, 0.f) + 1e-5f), am = -a * mu;
        if (t0 + er < SEQ) {
          float* op = out + (t0 + er) * DM + 64 * g + 8 * ei;
#pragma unroll
          for (int k = 0; k < 4; ++k) {
            cf o0 = pfma(pfma(z[2 * k], cf{a, a}, cf{am, am}), G[2 * k], z[2 * k] + piv);
            cf o1 = pfma(pfma(z[2 * k + 1], cf{a, a}, cf{am, am}), G[2 * k + 1], z[2 * k + 1] + piv);
            *(f32x4*)(op + (k >> 1) * 32 + (k & 1) * 4) = f32x4{o0.x, o0.y, o1.x, o1.y};
          }
        }
      }
    }
    if (L == 0) {
      BAR();
      STAMP(9);
    }
  }
  if (me == 25 * 9 && tid == 0) st8(tag, rs, 0, 0);
  STAMP(16);
  DUMP();
}

extern "C" void kernel_launch(void* const* d_in, const int* in_sizes, int n_in, void* d_out, int out_size, void* d_ws,
                              size_t ws_size, hipStream_t stream) {
  const u16* const* in = (const u16* const*)d_in;
  fused_kernel<<<256, 512, 0, stream>>>(in[0], in[1], in[2], in[3], in[4], in[5], in[6], in[7], in[8], in[10], in[11],
                                        in[12], in[13], in[14], (float*)d_out, (char*)d_ws);
}
```

```cpp
#include <hip/hip_runtime.h>
#include <hip/hip_bf16.h>
#include <stdint.h>

typedef __bf16 bf16x8 __attribute__((ext_vector_type(8)));
typedef __bf16 bf16x2 __attribute__((ext_vector_type(2)));
typedef float f32x4 __attribute__((ext_vector_type(4)));
typedef float f32x2 __attribute__((ext_vector_type(2)));
typedef unsigned u32x4 __attribute__((ext_vector_type(4)));
typedef unsigned u32x2 __attribute__((ext_vector_type(2)));
typedef unsigned short u16;
typedef __amdgpu_buffer_rsrc_t rsrc_t;
#define DEV __device__ __forceinline__

constexpr int SEQ = 2048, DM = 576, TC = 80, NP = 26, NG = 9;
constexpr unsigned WS_KVF = 4096;
constexpr unsigned WS_GF = WS_KVF + 9 * 256;
constexpr unsigned WS_BLK = 32768;
constexpr unsigned BLK = 4352, BLK_S1 = 1024, BLK_S2 = 3584, BLK_CH = 264;
constexpr unsigned WS_RKV = WS_BLK + 234 * BLK;
constexpr unsigned WS_GT = WS_RKV + DM * 4096;
constexpr unsigned WS_HT = WS_GT + DM * 4096;
constexpr unsigned WS_XT = WS_HT + 2 * 234 * 20480;
constexpr unsigned WS_STAMP = WS_XT + 234 * 10240;
constexpr unsigned WS_W2 = WS_STAMP + 65536;
constexpr unsigned WS_END = WS_W2 + 81 * 16384;
constexpr unsigned RB = DM * 2, OOB = 0x80000000u, CODE_PF = 38400;
constexpr int L_K = 92160, L_R = 0, L_X = 16384, L_P = 40960, L_H = 81920, L_B = 122880, L_G = 143360, L_M = 158720;
constexpr int SPIN = 1 << 16;
constexpr unsigned SENT = 0xFFFFFFFFu;

#define BAR() asm volatile("s_waitcnt lgkmcnt(0)\n\ts_barrier" ::: "memory")
#define DRAIN() asm volatile("s_waitcnt vmcnt(0)" ::: "memory")
#define STAMP(i)
#define DUMP()

DEV unsigned pk(float lo, float hi) { f32x2 v = {lo, hi}; return __builtin_bit_cast(unsigned, __builtin_convertvector(v, bf16x2)); }
DEV float lo16(unsigned u) { return __uint_as_float(u << 16); }
DEV float hi16(unsigned u) { return __uint_as_float(u & 0xffff0000u); }
DEV float rb(float x) { return lo16(pk(x, x)); }
DEV float b2f(u16 h) { return __uint_as_float((unsigned)h << 16); }
template <int C> DEV float dpp(float v) { return __int_as_float(__builtin_amdgcn_update_dpp(0, __float_as_int(v), C, 0xf, 0xf, true)); }
DEV float dot2(unsigned a, unsigned b, float c) { return __builtin_amdgcn_fdot2_f32_bf16(__builtin_bit_cast(bf16x2, a), __builtin_bit_cast(bf16x2, b), c, false); }
DEV f32x4 mfma(u32x4 a, u32x4 b, f32x4 c) { return __builtin_amdgcn_mfma_f32_16x16x32_bf16(__builtin_bit_cast(bf16x8, a), __builtin_bit_cast(bf16x8, b), c, 0, 0, 0); }
DEV unsigned canon(float v) { unsigned u = __float_as_uint(v); return u == SENT ? 0x7fc00000u : u; }
typedef f32x2 cf;
DEV cf pfma(cf a, cf b, cf c) { return __builtin_elementwise_fma(a, b, c); }
DEV cf cfma(cf a, cf b, cf c) { return pfma(cf{-a.y, a.y}, b.yx, pfma(a.xx, b, c)); }
DEV cf cmul(cf a, cf b) { return pfma(cf{-a.y, a.y}, b.yx, a.xx * b); }
DEV cf csel(bool p, cf a, cf b) { return {p ? a.x : b.x, p ? a.y : b.y}; }
DEV cf up2(unsigned u) { return {lo16(u), hi16(u)}; }
DEV unsigned pk2(cf v) { return __builtin_bit_cast(unsigned, __builtin_convertvector(v, bf16x2)); }
DEV cf sw16(float v) { return __builtin_bit_cast(cf, __builtin_amdgcn_permlane16_swap(__float_as_uint(v), __float_as_uint(v), false, false)); }
DEV cf sw32(float v) { return __builtin_bit_cast(cf, __builtin_amdgcn_permlane32_swap(__float_as_uint(v), __float_as_uint(v), false, false)); }
DEV float red16(float v) { cf t = sw16(v); return t.x + t.y; }
DEV float red32(float v) { cf t = sw32(v); return t.x + t.y; }
#define PIN(x) asm volatile("" : "+v"(x))
#define FENCE() do { asm volatile("" ::: "memory"); __builtin_amdgcn_sched_barrier(0); } while (0)
DEV rsrc_t mkrs(const void* p, unsigned n) { return __builtin_amdgcn_make_buffer_rsrc((void*)p, 0, n, 0x00020000); }
DEV u32x4 in16(rsrc_t rs, unsigned v, unsigned s) { return __builtin_amdgcn_raw_buffer_load_b128(rs, v, s, 0); }
DEV unsigned in4(rsrc_t rs, unsigned v) { return __builtin_amdgcn_raw_buffer_load_b32(rs, v, 0, 0); }
DEV u32x4 ld16(rsrc_t rs, unsigned v, unsigned s) { return __builtin_amdgcn_raw_buffer_load_b128(rs, v, s, 16); }
DEV u32x2 ld8(rsrc_t rs, unsigned v, unsigned s) { return __builtin_amdgcn_raw_buffer_load_b64(rs, v, s, 16); }
DEV unsigned ld4(rsrc_t rs, unsigned v, unsigned s) { return __builtin_amdgcn_raw_buffer_load_b32(rs, v, s, 16); }
DEV void st16(u32x4 d, rsrc_t rs, unsigned v, unsigned s) { __builtin_amdgcn_raw_buffer_store_b128(d, rs, v, s, 16); }
DEV void st8(u32x2 d, rsrc_t rs, unsigned v, unsigned s) { __builtin_amdgcn_raw_buffer_store_b64(d, rs, v, s, 16); }
DEV void st4(unsigned d, rsrc_t rs, unsigned v, unsigned s) { __builtin_amdgcn_raw_buffer_store_b32(d, rs, v, s, 16); }
DEV int wrap9(int v) { return v >= 9 ? v - 9 : v; }
DEV int gof(int g, int rot, int i) { return i == 0 ? g : wrap9(g + 1 + ((i - 1 + rot) & 7)); }
DEV int fsw(int row) { return ((row / 20) << 1) | ((row >> 1) & 1); }

DEV void poll_flags(rsrc_t rs, unsigned off, unsigned vo, u32x2 tag) {
#pragma unroll 1
  for (int it = 0; it < SPIN; ++it) {
    u32x2 v = ld8(rs, vo, off);
    if (__all(v.x == tag.x && v.y == tag.y)) break;
    asm volatile("s_sleep 1" ::: "memory");
  }
  asm volatile("" ::: "memory");
}

DEV void build_k(char* lds, int tid, u32x4 lo, u32x4 hi) {
  unsigned w[8] = {lo.x, lo.y, lo.z, lo.w, hi.x, hi.y, hi.z, hi.w}, o[7];
#pragma unroll
  for (int k = 0; k < 7; ++k) o[k] = __builtin_amdgcn_alignbit(w[k + 1], w[k], 16);
  char* p = lds + L_K + (tid >> 8) * 33280 + ((tid & 255) + 1) * 16;
#pragma unroll
  for (int s = 0; s < 8; ++s) {
    u32x4 v;
    if (s & 1) v = u32x4{w[(s + 1) / 2], w[(s + 1) / 2 + 1], w[(s + 1) / 2 + 2], w[(s + 1) / 2 + 3]};
    else v = u32x4{o[s / 2], o[s / 2 + 1], o[s / 2 + 2], o[s / 2 + 3]};
    *(u32x4*)(p + s * 4160) = v;
  }
  if (tid < 16) *(u32x4*)(lds + L_K + (tid >> 3) * 33280 + (tid & 7) * 4160) = u32x4{0, 0, 0, 0};
}
DEV void load_k(const u16* ssk, int d0, int tid, u32x4& lo, u32x4& hi) {
  const u16* kr = ssk + (size_t)(d0 + (tid >> 8)) * SEQ;
  int ci = tid & 255;
  hi = *(const u32x4*)(kr + 8 * ci);
  lo = *(const u32x4*)(kr + 8 * (ci > 0 ? ci - 1 : 0));
}
template <int SET, class FN> DEV void conv_mfma(const char* kb, const char* ub, f32x4 (&Y)[4], FN side) {
  constexpr int C0 = SET ? 1 : 0, C1 = SET ? 2 : 3, C2 = SET ? 4 : 5, C3 = SET ? 7 : 6, N = C3 + 1;
  u32x4 U[N], K[3];
#pragma unroll
  for (int p = C3; p >= 0; --p) U[p] = *(const u32x4*)(ub + 512 * (7 - p));
#pragma unroll
  for (int a = 0; a < 3; ++a) K[a] = *(const u32x4*)(kb + 512 * a);
  __builtin_amdgcn_sched_barrier(0);
#pragma unroll
  for (int st = 0; st < 4 * N; ++st) {
    const int rr = st / N, a = st % N, nx = st + 3;
    Y[3] = mfma(K[st % 3], U[C3 - a], Y[3]);
    if (C2 >= a) Y[2] = mfma(K[st % 3], U[C2 >= a ? C2 - a : 0], Y[2]);
    if (C1 >= a) Y[1] = mfma(K[st % 3], U[C1 >= a ? C1 - a : 0], Y[1]);
    if (C0 >= a) Y[0] = mfma(K[st % 3], U[C0 >= a ? C0 - a : 0], Y[0]);
    if (nx < 4 * N) K[st % 3] = *(const u32x4*)(kb + 512 * (nx % N) + 64 * (nx / N));
    if (rr < 3) U[C3 - a] = *(const u32x4*)(ub + 512 * (7 - (C3 - a)) + 64 * (rr + 1));
    side(st);
    __builtin_amdgcn_sched_barrier(0);
  }
}
template <class FN> DEV void conv_pair(char* lds, rsrc_t rs, int d0, int tid, int w, u32x4 rv, float dd, FN mid) {
  int l = tid & 63, chn = w >> 2, set = w & 1, rh = (w >> 1) & 1;
  if ((tid & 255) < 32) *(u32x4*)(lds + L_R + chn * 4608 + 4096 + l * 16) = u32x4{0, 0, 0, 0};
  *(u32x4*)(lds + L_R + chn * 4608 + (tid & 255) * 16) = rv;
  BAR();
  STAMP(20);
  int b = l & 15, g = l >> 4;
  const char* kb = lds + L_K + chn * 33280 + (b & 7) * 4160 + (g + (b >> 3)) * 16 + rh * 256;
  const char* ub = lds + L_R + chn * 4608 + (254 - 2 * b + g) * 16 - 3584 + rh * 256;
  f32x4 Y[4] = {};
  if (set) conv_mfma<1>(kb, ub, Y, mid); else conv_mfma<0>(kb, ub, Y, mid);
  STAMP(21);
  mid(-1);
  STAMP(22);
  f32x4 keep[2];
#pragma unroll
  for (int k = 0; k < 2; ++k) {
    f32x4 snd;
#pragma unroll
    for (int m = 0; m < 4; ++m) {
      float a = Y[k][m], bb = Y[2 + k][m];
      asm volatile("" : "+v"(a), "+v"(bb));
      snd[m] = rh ? a : bb; keep[k][m] = rh ? bb : a;
    }
    *(f32x4*)(lds + L_X + w * 2048 + k * 1024 + l * 16) = snd;
  }
  BAR();
  STAMP(23);
  u32x2 og[2];
#pragma unroll
  for (int k = 0; k < 2; ++k) {
    f32x4 y = keep[k] + *(const f32x4*)(lds + L_X + (w ^ 2) * 2048 + k * 1024 + l * 16);
    int cb = ((set ? 0x7421 : 0x6530) >> (4 * (2 * rh + k))) & 15;
    int tb = 256 * cb + 16 * b + 4 * g;
    u32x2 kv = *(const u32x2*)(lds + L_R + chn * 4608 + (2044 - tb) * 2);
    unsigned y01 = pk(y.x, y.y), y23 = pk(y.z, y.w);
    unsigned s01 = pk(hi16(kv.y) * dd, lo16(kv.y) * dd), s23 = pk(hi16(kv.x) * dd, lo16(kv.x) * dd);
    og[k] = u32x2{pk(lo16(y01) + lo16(s01), hi16(y01) + hi16(s01)), pk(lo16(y23) + lo16(s23), hi16(y23) + hi16(s23))};
  }
  u32x2 sx = __builtin_amdgcn_permlane16_swap(og[0].x, og[1].x, false, false), sy = __builtin_amdgcn_permlane16_swap(og[0].y, og[1].y, false, false);
  int cb = ((set ? 0x7421 : 0x6530) >> (4 * (2 * rh + (g & 1)))) & 15;
  st16(u32x4{sx.x, sy.x, sx.y, sy.y}, rs, 512 * cb + 32 * b + 16 * (g >> 1), WS_GT + (d0 + chn) * 4096);
}
constexpr int RG = 4;
template <int KIND> DEV u32x4 frag(const char* lds, int fb, int s) {
  if (KIND == 1) return *(const u32x4*)(lds + (fb ^ (((s / 5) & 1) * 64)) + (s / 10) * 10240 + (s % 5) * 512);
  return *(const u32x4*)(lds + (fb ^ (((s / 5) & 1) * 16)) + ((s / 10 + 5) % 6 + (s < 10 ? -1 : 0)) * 20480 + (s % 5) * 4096);
}
template <int KIND, int S0, int S1, class FN>
DEV void gsteps(const char* lds, int fb, const u32x4 (&W)[18], f32x4 (&acc)[5], u32x4 (&F)[RG + 1], FN side) {
#pragma unroll
  for (int s = S0; s < S1; ++s) {
    const int m = s - RG;
    if (s < 90) F[s % (RG + 1)] = frag<KIND>(lds, fb, s);
    __builtin_amdgcn_sched_barrier(0);
    if (m >= 0) acc[m % 5] = KIND == 1 ? mfma(F[m % (RG + 1)], W[m / 5], acc[m % 5]) : mfma(W[m / 5], F[m % (RG + 1)], acc[m % 5]);
    side(s);
    __builtin_amdgcn_sched_barrier(0);
  }
}
DEV void ldb(rsrc_t rs, u32x4* T, unsigned so, int l) {
#pragma unroll
  for (int n = 0; n < 5; ++n) T[n] = ld16(rs, l * 16 + n * 1024, so);
}
DEV void need(rsrc_t rs, u32x4* T, unsigned so, int l) {
#pragma unroll 1
  for (int it = 0; it < SPIN; ++it) {
    unsigned m = 0;
#pragma unroll
    for (int n = 0; n < 5; ++n) m = max(max(max(m, T[n].x), T[n].y), max(T[n].z, T[n].w));
    if (!__any(m == SENT)) break;
    asm volatile("" ::: "memory");
    ldb(rs, T, so, l);
  }
}
#define PINS(x) asm volatile("" : "+s"(x))

__global__ __launch_bounds__(512, 2) void fused_kernel(
    const u16* __restrict__ X, const u16* __restrict__ Lre, const u16* __restrict__ Lim, const u16* __restrict__ Bre,
    const u16* __restrict__ Bim, const u16* __restrict__ Cre, const u16* __restrict__ Cim, const u16* __restrict__ logdt,
    const u16* __restrict__ Dsk, const u16* __restrict__ Kin, const u16* __restrict__ Vin, const u16* __restrict__ shk,
    const u16* __restrict__ ssk, const u16* __restrict__ sD, float* __restrict__ out, char* ws) {
  __shared__ __attribute__((aligned(16))) char lds[163840];
  int tid = threadIdx.x, l = tid & 63;
  const int w = __builtin_amdgcn_readfirstlane(tid >> 6);
  const int xcd = blockIdx.x & 7, loc = blockIdx.x >> 3;
  int c, g, spare = -1;
  if (loc < 27) { c = 3 * xcd + loc / 9; g = loc % 9; }
  else {
    int idx = xcd * 5 + loc - 27;
    if (idx >= 36) return;
    if (idx < 18) { c = 24 + idx / 9; g = idx % 9; } else { spare = idx - 18; c = 0; g = spare >> 1; }
  }
  asm volatile("" : "+s"(ws));
  unsigned long long pc0;
  asm volatile("s_getpc_b64 %0" : "=s"(pc0));
  const rsrc_t rs = __builtin_amdgcn_make_buffer_rsrc(ws, 0, WS_END, 0x00020000);
  for (int i = 30; i >= 0; --i) STAMP(i);
#define GETTAG() do { tag.x = __builtin_amdgcn_readfirstlane(tag.x) + 1; tag.y = __builtin_amdgcn_readfirstlane(tag.y) + 1; } while (0)
  if (spare >= 0) {
    u32x2 tag = ld8(rs, 0, 0);
    const int d0 = 64 * g + 2 * (26 + 3 * (spare & 1));
    u32x4 klo[3], khi[3], rv[3];
    {
      const rsrc_t r0 = mkrs(Cre, DM * RB), r1 = mkrs(Cim, DM * RB);
      const unsigned dro = (64 * g + 16 * (w & 3) + (l & 15)) * RB + 64 * (w >> 2) + 16 * (l >> 4);
      const int i0 = 4 * (spare & 1);
      u32x4 re[5], im[5];
#pragma unroll
      for (int i = 0; i < 5; ++i) { re[i] = in16(r0, dro, (i0 + i) * 128); im[i] = in16(r1, dro, (i0 + i) * 128); }
#pragma unroll
      for (int i = 0; i < 5; ++i)
#pragma unroll
        for (int k = 0; k < 2; ++k) {
          unsigned a = re[i][2 * k], b = im[i][2 * k] ^ 0x80008000u, a2 = re[i][2 * k + 1], b2 = im[i][2 * k + 1] ^ 0x80008000u;
          u32x4 o = {__builtin_amdgcn_perm(b, a, 0x05040100), __builtin_amdgcn_perm(b, a, 0x07060302),
                     __builtin_amdgcn_perm(b2, a2, 0x05040100), __builtin_amdgcn_perm(b2, a2, 0x07060302)};
          if (i > 0 || i0 == 0) st16(o, rs, l * 16 + k * 1024, WS_W2 + ((g * 9 + i0 + i) * 8 + w) * 2048);
        }
    }
    GETTAG();
    DRAIN();
    BAR();
    if (tid == 0) st8(tag, rs, 0, WS_KVF + g * 256 + (NP + (spare & 1)) * 8);
#pragma unroll
    for (int r = 0; r < 3; ++r) load_k(ssk, d0 + 2 * r, tid, klo[r], khi[r]);
#pragma unroll 1
    for (int r = 0; r < 3; ++r) {
      if ((tid & 255) == 0) klo[0] = u32x4{0, 0, 0, 0};
      build_k(lds, tid, klo[0], khi[0]);
      if (r == 0) {
        poll_flags(rs, WS_KVF + g * 256, min(l, NP - 1) * 8, tag);
#pragma unroll
        for (int k = 0; k < 3; ++k) rv[k] = ld16(rs, (tid & 255) * 16, WS_RKV + (d0 + 2 * k + (w >> 2)) * 4096);
      }
      conv_pair(lds, rs, d0 + 2 * r, tid, w, rv[0], b2f(sD[d0 + 2 * r + (w >> 2)]), [](int) {});
      klo[0] = klo[1]; klo[1] = klo[2]; khi[0] = khi[1]; khi[1] = khi[2]; rv[0] = rv[1]; rv[1] = rv[2];
      BAR();
    }
    DRAIN();
    BAR();
    if (tid == 0) st8(tag, rs, 0, WS_GF + g * 256 + (26 + (spare & 1)) * 8);
    STAMP(1);
    DUMP();
    return;
  }
  asm volatile("" ::: "memory");
  u32x2 tag = ld8(rs, 0, 0);
  const int t0 = TC * c, me = c * 9 + g, rot = 3 * (c % 3);
  int q = l >> 4, ri = l & 1, pp = (l & 15) >> 1, s8 = 2 * q + ri, er, ei, eo0;
#define REDERIVE() do { asm volatile("" : "+v"(tid)); l = tid & 63; q = l >> 4; ri = l & 1; pp = (l & 15) >> 1; s8 = 2 * q + ri; \
    er = tid >> 2; ei = l & 3; eo0 = er * 128 + ((ei ^ fsw(er)) << 4); } while (0)
  const u32x4 S4 = {SENT, SENT, SENT, SENT};
  auto clr = [&](unsigned so, int n) {
#pragma unroll
    for (int k = 0; k < n; k += 512) st16(S4, rs, k + 512 <= n || tid < n - k ? tid * 16 : OOB, so + k * 16);
  };
  const rsrc_t rx = mkrs(X, SEQ * RB);
  unsigned kk[11], vv[8]; u32x4 shv;
  const int dp = l & 31, tb = t0 + 8 * (2 * w + (l >> 5));
  if (w < 5) {
    const rsrc_t rk = mkrs(Kin, SEQ * RB), rvv = mkrs(Vin, SEQ * RB);
    unsigned b1 = tb * RB + 128 * g + 4 * dp, b0 = tb ? b1 - 3 * RB : OOB, b2 = b1 + 4 * RB;
    PIN(b0); PIN(b1); PIN(b2);
    shv = *(const u32x4*)(shk + (64 * g + 2 * dp) * 4);
#pragma unroll
    for (int j = 0; j < 11; ++j) kk[j] = in4(rk, j < 3 ? b0 + j * RB : j < 7 ? b1 + (j - 3) * RB : b2 + (j - 7) * RB);
#pragma unroll
    for (int j = 0; j < 8; ++j) vv[j] = in4(rvv, j < 4 ? b1 + j * RB : b2 + (j - 4) * RB);
  } else {
    const rsrc_t rc = mkrs((const void*)(pc0 & ~127ull), CODE_PF), ro = mkrs(out, SEQ * DM * 4);
    kk[0] = in4(rc, (tid - 320) * 128);
    kk[1] = in4(rc, (tid - 128) * 128);
    kk[2] = in4(ro, tid < 400 ? (t0 + tid - 320) * (DM * 4) + 256 * g : OOB);
  }
  FENCE();
  const int p = 64 * g + 8 * w + pp;
  u32x4 W1[18];
  auto ldw1 = [&](int i0) {
    const rsrc_t r0 = mkrs(Bre, DM * RB), r1 = mkrs(Bim, DM * RB);
    const unsigned wo = (64 * g + 8 * w + (l >> 3)) * RB + (l & 7) * 16;
#pragma unroll
    for (int i = i0; i < 9; ++i) { W1[2 * i] = in16(r0, wo, gof(g, rot, i) * 128); W1[2 * i + 1] = in16(r1, wo, gof(g, rot, i) * 128); }
  };
  auto fixw1 = [&](int i0, int base) {
    const int sw = base + (l >> 3) * 128 + (((l & 7) ^ ((l >> 4) * 2)) << 4);
    const int sr = base + ri * 1024 + pp * 128 + ((q ^ ((pp >> 1) * 2 + ri)) << 4);
#pragma unroll
    for (int i = i0; i < 9; ++i) {
      *(u32x4*)(lds + sw + (i & 3) * 2048) = W1[2 * i];
      *(u32x4*)(lds + (sw ^ 16) + (i & 3) * 2048 + 1024) = W1[2 * i + 1];
      FENCE();
      W1[2 * i] = *(const u32x4*)(lds + sr + (i & 3) * 2048);
      W1[2 * i + 1] = *(const u32x4*)(lds + (sr ^ 64) + (i & 3) * 2048);
      FENCE();
    }
  };
  ldw1(0);
  FENCE();
  u32x4 xa[9], xb[3];
  const int row = tid >> 3, row2 = 64 + (tid >> 5), e = tid & 31;
  {
    unsigned xo = (t0 + row) * RB + (tid & 7) * 16, xo2 = (t0 + row2) * RB + (e & 7) * 16;
#pragma unroll
    for (int i = 0; i < 9; ++i) xa[i] = in16(rx, xo, gof(g, rot, i) * 128);
    xb[0] = in16(rx, xo2 + gof(g, rot, e >> 3) * 128, 0);
    xb[1] = in16(rx, xo2 + gof(g, rot, (e >> 3) + 4) * 128, 0);
    xb[2] = in16(rx, e < 8 ? xo2 : OOB, gof(g, rot, 8) * 128);
  }
  u32x4 klo, khi;
  load_k(ssk, 64 * g + 2 * c, tid, klo, khi);
  const u16 lre_ = Lre[p], lim_ = Lim[p], ldt_ = logdt[p];
  clr(WS_BLK + me * BLK, BLK_CH);
  clr(WS_HT + me * 20480, 1280);
  FENCE();
  if (w >= 5) asm volatile("" :: "v"(kk[0]), "v"(kk[1]), "v"(kk[2]));
  if (w < 5) {
    cf sh[4] = {{lo16(shv.x), lo16(shv.z)}, {hi16(shv.x), hi16(shv.z)}, {lo16(shv.y), lo16(shv.w)}, {hi16(shv.y), hi16(shv.w)}};
    cf P[8];
#pragma unroll
    for (int j = 0; j < 8; ++j) {
      cf a = up2(kk[j]) * sh[3];
#pragma unroll
      for (int tt = 1; tt < 4; ++tt) a = pfma(up2(kk[j + tt]), sh[3 - tt], a);
      P[j] = a * up2(vv[j]);
    }
    u32x4 o0, o1;
#pragma unroll
    for (int k = 0; k < 4; ++k) { o0[k] = pk(P[7 - 2 * k].x, P[6 - 2 * k].x); o1[k] = pk(P[7 - 2 * k].y, P[6 - 2 * k].y); }
    unsigned vo = tb < SEQ ? 2 * dp * 4096 + (2040 - tb) * 2 : OOB;
    st16(o0, rs, vo, WS_RKV + 64 * g * 4096);
    st16(o1, rs, vo, WS_RKV + (64 * g + 1) * 4096);
  }
  STAMP(17);
  FENCE();
  float cs;
  {
    fixw1(0, L_K + w * 8192);
    f32x4 ca = {};
#pragma unroll
    for (int i = 0; i < 18; ++i) ca = mfma(u32x4{0x3f803f80u, 0x3f803f80u, 0x3f803f80u, 0x3f803f80u}, W1[i], ca);
    cs = ca.x;
    PIN(cs);
  }
  FENCE();
  {
    float s1 = 0.f, s2 = 0.f, u1 = 0.f, u2 = 0.f;
    char* d = lds + row * 128 + (((tid & 7) ^ fsw(row)) << 4);
#pragma unroll
    for (int i = 0; i < 9; ++i) {
      *(u32x4*)(d + i * 10240) = xa[i];
#pragma unroll
      for (int k = 0; k < 4; ++k) { s1 = dot2(xa[i][k], 0x3f803f80u, s1); s2 = dot2(xa[i][k], xa[i][k], s2); }
    }
    char* d2 = lds + row2 * 128 + (((e & 7) ^ fsw(row2)) << 4) + (e >> 3) * 10240;
#pragma unroll
    for (int n = 0; n < 3; ++n) {
      if (n < 2 || e < 8) *(u32x4*)(d2 + n * 40960) = xb[n];
#pragma unroll
      for (int k = 0; k < 4; ++k) { u1 = dot2(xb[n][k], 0x3f803f80u, u1); u2 = dot2(xb[n][k], xb[n][k], u2); }
    }
    s1 += dpp<0xB1>(s1); s2 += dpp<0xB1>(s2); u1 += dpp<0xB1>(u1); u2 += dpp<0xB1>(u2);
    s1 += dpp<0x4E>(s1); s2 += dpp<0x4E>(s2); u1 += dpp<0x4E>(u1); u2 += dpp<0x4E>(u2);
    s1 += dpp<0x141>(s1); s2 += dpp<0x141>(s2); u1 += dpp<0x141>(u1); u2 += dpp<0x141>(u2);
    u1 += dpp<0x140>(u1); u2 += dpp<0x140>(u2);
    u1 = red16(u1); u2 = red16(u2);
    bool ex = (tid & 31) == 1;
    float a1 = (ex ? u1 : s1) * (1.f / DM), a2 = (ex ? u2 : s2) * (1.f / DM);
    float rsd = __builtin_amdgcn_rcpf(rb(__builtin_amdgcn_sqrtf(rb(rb(fmaxf(a2 - a1 * a1, 0.f)) + 1e-5f))));
    if (ex || (tid & 7) == 0) *(f32x2*)(lds + L_M + (ex ? row2 : row) * 8) = f32x2{rsd, -rb(a1)};
  }
  STAMP(18);
  GETTAG();
  float lre = b2f(lre_), lim = b2f(lim_), ldt = b2f(ldt_);
  cf lam, co;
  {
    float dt = rb(__expf(ldt)), ee = __expf(dt * lre), rev = __builtin_amdgcn_fractf(dt * lim * 0.15915494309189535f);
    lam = cf{__builtin_amdgcn_cosf(rev), __builtin_amdgcn_sinf(rev)} * ee;
    float den = 2.f * __builtin_amdgcn_rcpf(lre * lre + lim * lim), nr = lam.x - 1.f, ni = lam.y;
    co = {(nr * lre + ni * lim) * den, (ni * lre - nr * lim) * den};
    *(f32x4*)(lds + L_M + 1024 + (8 * w + pp) * 16) = f32x4{lam.x, lam.y, co.x, co.y};
  }
  DRAIN();
  BAR();
  if (tid == 0) st8(tag, rs, 0, WS_KVF + g * 256 + c * 8);
  STAMP(1);
  if ((tid & 255) == 0) klo = u32x4{0, 0, 0, 0};
  build_k(lds, tid, klo, khi);
  STAMP(19);
  u32x4 W2[18];
  constexpr int W2A = 5, DLY_X = 8, DLY_H0 = 8, DLY_H1 = 12;
  auto ldw2 = [&](int i0, int i1) {
#pragma unroll
    for (int i = i0; i < i1; ++i) {
      const unsigned so = WS_W2 + ((g * 9 + gof(g, rot, i)) * 8 + w) * 2048;
      W2[2 * i] = ld16(rs, l * 16, so); W2[2 * i + 1] = ld16(rs, l * 16 + 1024, so);
    }
  };

#pragma unroll
  for (int L = 0; L < 2; ++L) {
    f32x4 acc[5] = {};
    u32x4 F[RG + 1];
    REDERIVE();
    const int ab = (20 * ((l & 15) >> 2) + (l & 3)) * 128 + ((q ^ ((l & 15) >> 1)) << 4);
    cf l10, l20;
    if (L) fixw1(8, 102400 + w * 2048);
    if (L) { f32x4 t = *(const f32x4*)(lds + L_M + 1024 + (8 * w + pp) * 16); lam = t.xy; co = t.zw; }
    cf cA = csel(ri, cf{-co.y, co.x}, co), cB = csel(ri, co, cf{-co.y, co.x});
    PIN(cA); PIN(cB);
    {
      cf l2 = cmul(lam, lam), l4 = cmul(l2, l2), l5 = cmul(l4, lam);
      l10 = cmul(l5, l5); l20 = cmul(l10, l10);
    }
    PIN(l10); PIN(l20);
    cf CS;
    u32x4 rv;
    if (L == 0) {
      u32x2 fl;
      unsigned fa = g * 256, fb = c * 8;
      PINS(fa); PINS(fb);
      const unsigned fo = l < NP + 2 ? fa + l * 8 : min(l - NP - 2, 8) * 256 + fb;
      gsteps<1, 0, 90 + RG>(lds, ab, W1, acc, F, [&](int s) {
        if (s == 60) fl = ld8(rs, fo, WS_KVF);
      });
      STAMP(2);
      if (!__all(fl.x == tag.x && fl.y == tag.y)) poll_flags(rs, WS_KVF, fo, tag);
      STAMP(3);
      rv = ld16(rs, (tid & 255) * 16, WS_RKV + (64 * g + 2 * c + (w >> 2)) * 4096);
      FENCE();
      CS = pfma(cA, cf{cs, cs}, cB * dpp<0xB1>(cs));
    } else {
      u32x4 T[10];
      unsigned sA = WS_XT + (c * 9 + gof(g, rot, 1 + (w >> 1))) * 10240 + (w & 1) * 5120;
      unsigned sB = WS_XT + (c * 9 + gof(g, rot, 5 + (w >> 1))) * 10240 + (w & 1) * 5120;
      char* td = lds + w * 5120 + l * 16;
      gsteps<1, 0, 10>(lds, ab, W1, acc, F, [&](int s) {});
      asm volatile("s_sleep %0" :: "n"(DLY_X) : "memory");
      ldb(rs, T, sA, l); ldb(rs, T + 5, sB, l);
      need(rs, T, sA, l);
      STAMP(10);
#pragma unroll
      for (int n = 0; n < 5; ++n) *(u32x4*)(td + 10240 + n * 1024) = T[n];
      BAR();
      gsteps<1, 10, 50>(lds, ab, W1, acc, F, [&](int s) {
        if (s == 16) need(rs, T + 5, sB, l);
        if (s % 4 == 0 && s >= 16 && s < 36) *(u32x4*)(td + 51200 + (s / 4 - 4) * 1024) = T[5 + (s / 4 - 4) % 5];
      });
      BAR();
      gsteps<1, 50, 90 + RG>(lds, ab, W1, acc, F, [&](int s) {});
      STAMP(11);
    }
    REDERIVE();
    cf u[10], hin;
    {
#pragma unroll
      for (int j = 0; j < 10; ++j) {
        float lo = acc[j >> 2][j & 3], hi = acc[(10 + j) >> 2][(10 + j) & 3];
        float kept = ri ? hi : lo, rcv = dpp<0xB1>(ri ? lo : hi);
        if (L == 0) {
          cf ms = *(const cf*)(lds + L_M + (10 * s8 + j) * 8);
          u[j] = pfma(cA, cf{kept, kept}, pfma(cB, cf{rcv, rcv}, CS * ms.y)) * ms.x;
        } else {
          u[j] = pfma(cA, cf{kept, kept}, cB * rcv);
        }
      }
      cf h = u[0];
#pragma unroll
      for (int j = 1; j < 10; ++j) h = cfma(lam, h, u[j]);
      cf o = {dpp<0xB1>(h.x), dpp<0xB1>(h.y)};
      cf ev = csel(ri, o, h), od = csel(ri, h, o);
      cf P = cfma(l10, ev, od);
      cf ax = sw16(P.x), ay = sw16(P.y);
      cf Pe = {ax.x, ay.x}, Po = {ax.y, ay.y}, D = cfma(l20, Pe, Po);
      cf bx = sw32(D.x), by = sw32(D.y), S2 = {bx.x, by.x}, l40 = cmul(l20, l20);
      cf E = cfma(l40, S2, cf{bx.y, by.y});
      if (s8 == 0) st8(u32x2{canon(E.x), canon(E.y)}, rs, (8 * w + pp) * 8, WS_BLK + me * BLK + L * 512);
      cf Sb = csel(q & 2, S2, cf{0.f, 0.f}), Sq = csel(q & 1, cfma(l20, Sb, Pe), Sb);
      hin = csel(ri, cfma(l10, Sq, ev), Sq);
    }
    u32x2 ev[4];
    const unsigned evs = WS_BLK + g * BLK + L * 512;
    auto ldev = [&] {
      const unsigned evo = (c - 1 - s8) * (9 * BLK) + (8 * w + pp) * 8;
#pragma unroll
      for (int i = 0; i < 4; ++i) ev[i] = ld8(rs, evo - i * (72 * BLK), evs);
    };
    BAR();
    if (L == 0) {
      STAMP(26);
      conv_pair(lds, rs, 64 * g + 2 * c, tid, w, rv, b2f(sD[64 * g + 2 * c + (w >> 2)]), [&](int st) {
        if (st < 0) { REDERIVE(); ldev(); ldw2(W2A, 9); FENCE(); }
        else if (st % 4 == 1 && st / 4 < W2A) ldw2(st / 4, st / 4 + 1);
      });
      STAMP(4);
    } else {
      ldev();
    }
    REDERIVE();
    {
      cf one = {1.f, 0.f}, l40 = cmul(l20, l20), l80 = cmul(l40, l40), l160 = cmul(l80, l80), l320 = cmul(l160, l160);
      cf l640 = cmul(l320, l320);
      cf l10s = cmul(cmul(csel(ri, l10, one), csel(q & 1, l20, one)), csel(q & 2, l40, one));
      cf l80s = cmul(cmul(csel(ri, l80, one), csel(q & 1, l160, one)), csel(q & 2, l320, one));
      int ha[10];
#pragma unroll
      for (int j = 0; j < 10; ++j) {
        int t = 10 * s8 + j;
        ha[j] = L_H + t * 256 + (((2 * w + (pp >> 2)) ^ (t & 15)) << 4) + (pp & 3) * 4;
        PIN(ha[j]);
      }
      PIN(l640); PIN(l10s); PIN(l80s);
#pragma unroll 1
      for (int it = 0; it < SPIN; ++it) {
        bool ok = true;
#pragma unroll
        for (int i = 0; i < 4; ++i) ok = ok && ev[i].x != SENT && ev[i].y != SENT;
        if (__all(ok)) break;
        asm volatile("" ::: "memory");
        ldev();
      }
      cf H = __builtin_bit_cast(cf, ev[3]);
#pragma unroll
      for (int i = 2; i >= 0; --i) H = cfma(l640, H, __builtin_bit_cast(cf, ev[i]));
      H = cmul(l80s, H);
      H.x += dpp<0xB1>(H.x); H.y += dpp<0xB1>(H.y);
      H = {red32(red16(H.x)), red32(red16(H.y))};
      hin = cfma(l10s, H, hin);
      STAMP(5 + 7 * L);
#pragma unroll
      for (int j = 0; j < 10; ++j) {
        hin = cfma(lam, hin, u[j]);
        *(unsigned*)(lds + ha[j]) = pk2(hin);
      }
    }
    BAR();
    const unsigned hbase = WS_HT + (L * NP + c) * 9 * 20480;
#pragma unroll
    for (int n = 0; n < 3; ++n)
      if (n < 2 || w < 4) st16(*(const u32x4*)(lds + L_H + tid * 16 + n * 8192), rs, tid * 16 + n * 8192, hbase + g * 20480);
    STAMP(6 + 7 * L);
    if (L == 0) {
      u32x4 xs[4];
      xs[0] = in16(rx, (t0 + er) * RB + 16 * ei, 128 * g); xs[1] = in16(rx, (t0 + er) * RB + 16 * ei + 64, 128 * g);
      xs[2] = *(const u32x4*)(Dsk + 64 * g + 8 * ei); xs[3] = *(const u32x4*)(Dsk + 64 * g + 32 + 8 * ei);
      FENCE();
      if (w < 5) {
        cf ms = *(const cf*)(lds + L_M + er * 8);
#pragma unroll
        for (int k = 0; k < 4; ++k) {
          cf x0 = up2(xs[k >> 1][2 * (k & 1)]) + ms.y, x1 = up2(xs[k >> 1][2 * (k & 1) + 1]) + ms.y;
          x0 = pfma(x0 * ms.x, up2(xs[2 + (k >> 1)][2 * (k & 1)]), x0);
          x1 = pfma(x1 * ms.x, up2(xs[2 + (k >> 1)][2 * (k & 1) + 1]), x1);
          *(f32x4*)(lds + L_B + k * 5120 + tid * 16) = f32x4{x0.x, x0.y, x1.x, x1.y};
        }
      }
    }
    cf G[8], z[8];
    u32x4 xs[4];
    {
      f32x4 y[5] = {};
      REDERIVE();
      const int hb = (l & 15) * 256 + (((8 * (w >> 2) + 2 * q) ^ (l & 15)) << 4);
      u32x4 T[10], gv[2];
      u32x2 fl;
      unsigned so[4];
#pragma unroll
      for (int k = 0; k < 4; ++k) so[k] = hbase + gof(g, rot, 1 + 2 * k + (w >> 2)) * 20480 + (w & 3) * 5120;
      char* td = lds + w * 5120 + l * 16;
      gsteps<2, 0, 10>(lds, hb, W2, y, F, [&](int s) {});
      asm volatile("s_sleep %0" :: "n"(L ? DLY_H1 : DLY_H0) : "memory");
      ldb(rs, T, so[0], l); ldb(rs, T + 5, so[1], l);
      if (L == 0) { FENCE(); clr(WS_XT + me * 10240, 640); clr(WS_HT + (234 + me) * 20480, 1280); }
      need(rs, T, so[0], l);
      STAMP(7 + 7 * L);
#pragma unroll
      for (int n = 0; n < 5; ++n) { *(u32x4*)(td + n * 1024) = T[n]; T[n] = ld16(rs, l * 16 + n * 1024, so[2]); }
      BAR();
      if (L == 0 && tid == 0) st8(tag, rs, 0, WS_GF + g * 256 + c * 8);
      gsteps<2, 10, 30>(lds, hb, W2, y, F, [&](int s) {
        if (s == 12) need(rs, T + 5, so[1], l);
        if (s % 3 == 0 && s < 27) { const int n = (s / 3 - 4) % 5; *(u32x4*)(td + 40960 + n * 1024) = T[5 + n]; T[5 + n] = ld16(rs, l * 16 + n * 1024, so[3]); }
      });
      BAR();
      gsteps<2, 30, 50>(lds, hb, W2, y, F, [&](int s) {
        if (s == 33) need(rs, T, so[2], l);
        if (s % 3 == 0 && s >= 33 && s < 48) { const int n = (s / 3 - 11) % 5; *(u32x4*)(td + 81920 + n * 1024) = T[n]; }
        if (L == 0 && s == 34) fl = ld8(rs, min(l, 27) * 8, WS_GF + g * 256);
      });
      if (L == 0) {
        if (!__all(fl.x == tag.x && fl.y == tag.y)) poll_flags(rs, WS_GF + g * 256, min(l, 27) * 8, tag);
#pragma unroll
        for (int n = 0; n < 2; ++n) {
          int id = min(tid + 512 * n, 639), dl = id / 10, ck = id - 10 * dl;
          gv[n] = ld16(rs, dl * 4096 + min(t0 + 8 * ck, SEQ - 8) * 2, WS_GT + 64 * g * 4096);
        }
      }
      BAR();
      gsteps<2, 50, 70>(lds, hb, W2, y, F, [&](int s) {
        if (s == 51) need(rs, T + 5, so[3], l);
        if (s % 3 == 0 && s < 66) { const int n = (s / 3 - 17) % 5; *(u32x4*)(td + n * 1024) = T[5 + n]; }
      });
      BAR();
      REDERIVE();
      if (L == 0) { xs[2] = *(const u32x4*)(Dsk + 64 * g + 8 * ei); xs[3] = *(const u32x4*)(Dsk + 64 * g + 32 + 8 * ei); }
      gsteps<2, 70, 90 + RG>(lds, hb, W2, y, F, [&](int s) {});
#pragma unroll
      for (int tt = 0; tt < 5; ++tt)
        *(f32x4*)(lds + L_P + (w >> 2) * 21760 + (16 * tt + (l & 15)) * 272 + (16 * (w & 3) + 4 * q) * 4) = y[tt];
      if (L == 0) {
#pragma unroll
        for (int n = 0; n < 2; ++n) {
          int id = min(tid + 512 * n, 639), dl = id / 10;
          *(u32x4*)(lds + L_G + id * 16 + ((dl >> 3) & 3) * 32 + (dl >> 5) * 128) = gv[n];
        }
        BAR();
      }
      {
        const char* gp = lds + L_G + 8 * ei * 160 + ei * 32 + er * 2;
#pragma unroll
        for (int k = 0; k < 4; ++k) { f32x4 b = *(const f32x4*)(lds + L_B + k * 5120 + tid * 16); z[2 * k] = b.xy; z[2 * k + 1] = b.zw; }
#pragma unroll
        for (int k = 0; k < 8; ++k) {
          int o = (k >> 2) * (32 * 160 + 128) + (k & 3) * 320;
          G[k] = {b2f(*(const u16*)(gp + o)), b2f(*(const u16*)(gp + o + 160))};
        }
      }
      if (L == 1) BAR();
    }
    STAMP(8 + 7 * L);
    if (L == 0) { ldw1(8); FENCE(); }
    if (w < 5) {
      const char* pb = lds + L_P + er * 272 + ei * 32;
      const float piv = *(const float*)(lds + L_M + er * 8 + 4 - 4 * L);
#pragma unroll
      for (int k = 0; k < 4; ++k) {
        int o = (k >> 1) * 128 + (k & 1) * 16;
        f32x4 a = *(const f32x4*)(pb + o), b = *(const f32x4*)(pb + 21760 + o);
        z[2 * k] += a.xy + b.xy; z[2 * k + 1] += a.zw + b.zw;
      }
      if (L == 0) {
        cf S[8] = {};
#pragma unroll
        for (int k = 0; k < 8; ++k) {
          cf zg = z[k] * G[k];
          S[0] += z[k]; S[1] = pfma(z[k], z[k], S[1]); S[2] += zg; S[3] = pfma(z[k], zg, S[3]);
          S[4] = pfma(zg, zg, S[4]); S[5] = pfma(zg, G[k], S[5]); S[6] += G[k]; S[7] = pfma(G[k], G[k], S[7]);
        }
        float s[8], r[4];
#pragma unroll
        for (int k = 0; k < 8; ++k) s[k] = S[k].x + S[k].y;
#pragma unroll
        for (int k = 0; k < 4; ++k) { const int a = (k >> 1) * 4 + (k & 1); r[k] = ((ei & 1) ? s[a + 2] : s[a]) + dpp<0xB1>((ei & 1) ? s[a] : s[a + 2]); }
        float v0 = ((ei & 2) ? r[2] : r[0]) + dpp<0x4E>((ei & 2) ? r[0] : r[2]);
        float v1 = ((ei & 2) ? r[3] : r[1]) + dpp<0x4E>((ei & 2) ? r[1] : r[3]);
        st8(u32x2{canon(v0), canon(v1)}, rs, tid * 8, WS_BLK + me * BLK + BLK_S1);
        STAMP(24);
        u32x2 sv[8];
#pragma unroll 1
        for (int it = 0; it < SPIN; ++it) {
          bool ok = true;
#pragma unroll
          for (int k = 0; k < 8; ++k) { sv[k] = ld8(rs, tid * 8, WS_BLK + (c * 9 + wrap9(g + 1 + k)) * BLK + BLK_S1); ok = ok && sv[k].x != SENT && sv[k].y != SENT; }
          if (__all(ok)) break;
          asm volatile("" ::: "memory");
        }
        STAMP(25);
        cf V = {v0, v1};
#pragma unroll
        for (int k = 0; k < 8; ++k) V += __builtin_bit_cast(cf, sv[k]);
        V *= 1.f / DM;
        float mu = dpp<0x00>(V.x), zz = dpp<0x00>(V.y), zG = dpp<0x55>(V.x), zzG = dpp<0x55>(V.y);
        float zzGG = dpp<0xAA>(V.x), zGG = dpp<0xAA>(V.y), G1 = dpp<0xFF>(V.x), G2 = dpp<0xFF>(V.y);
        float a = __builtin_amdgcn_rsqf(fmaxf(zz - mu * mu, 0.f) + 1e-5f);
        float mw = mu + a * (zG - mu * G1);
        float ww = zz + 2.f * a * (zzG - mu * zG) + a * a * (zzGG - 2.f * mu * zGG + mu * mu * G2);
        float a2 = __builtin_amdgcn_rsqf(fmaxf(ww - mw * mw, 0.f) + 1e-5f), am = -a * mu;
        u32x4 o[2];
#pragma unroll
        for (int k = 0; k < 8; ++k) {
          z[k] = pfma(pfma(z[k], cf{a, a}, cf{am, am}), G[k], z[k] - mw);
          G[k] = z[k] * a2;
          o[k >> 2][k & 3] = pk2(G[k]);
        }
        st16(o[0], rs, eo0, WS_XT + me * 10240);
        st16(o[1], rs, eo0 ^ 64, WS_XT + me * 10240);
        FENCE();
        *(float*)(lds + L_M + er * 8) = mw - piv;
        *(u32x4*)(lds + eo0) = o[0];
        *(u32x4*)(lds + (eo0 ^ 64)) = o[1];
#pragma unroll
        for (int k = 0; k < 4; ++k) {
          cf b0 = pfma(G[2 * k], up2(xs[2 + (k >> 1)][2 * (k & 1)]), z[2 * k]);
          cf b1 = pfma(G[2 * k + 1], up2(xs[2 + (k >> 1)][2 * (k & 1) + 1]), z[2 * k + 1]);
          *(f32x4*)(lds + L_B + k * 5120 + tid * 16) = f32x4{b0.x, b0.y, b1.x, b1.y};
        }
      } else {
        cf S0 = z[0], S1 = z[0] * z[0];
#pragma unroll
        for (int k = 1; k < 8; ++k) { S0 += z[k]; S1 = pfma(z[k], z[k], S1); }
        float s0 = S0.x + S0.y, s1 = S1.x + S1.y;
        s0 += dpp<0xB1>(s0); s1 += dpp<0xB1>(s1); s0 += dpp<0x4E>(s0); s1 += dpp<0x4E>(s1);
        if (ei == 0) st8(u32x2{canon(s0), canon(s1)}, rs, er * 8, WS_BLK + me * BLK + BLK_S2);
        u32x2 sv[2];
        const unsigned so[2] = {er * 8 + wrap9(g + 1 + ei) * BLK, er * 8 + wrap9(g + 5 + ei) * BLK};
#pragma unroll 1
        for (int it = 0; it < SPIN; ++it) {
          bool ok = true;
#pragma unroll
          for (int k = 0; k < 2; ++k) { sv[k] = ld8(rs, so[k], WS_BLK + c * 9 * BLK + BLK_S2); ok = ok && sv[k].x != SENT && sv[k].y != SENT; }
          if (__all(ok)) break;
          asm volatile("" ::: "memory");
        }
        cf V = __builtin_bit_cast(cf, sv[0]) + __builtin_bit_cast(cf, sv[1]);
        V.x += dpp<0xB1>(V.x); V.y += dpp<0xB1>(V.y); V.x += dpp<0x4E>(V.x); V.y += dpp<0x4E>(V.y);
        float mu = (s0 + V.x) * (1.f / DM), a = __builtin_amdgcn_rsqf(fmaxf((s1 + V.y) * (1.f / DM) - mu * mu, 0.f) + 1e-5f), am = -a * mu;
        if (t0 + er < SEQ) {
          float* op = out + (t0 + er) * DM + 64 * g + 8 * ei;
#pragma unroll
          for (int k = 0; k < 4; ++k) {
            cf o0 = pfma(pfma(z[2 * k], cf{a, a}, cf{am, am}), G[2 * k], z[2 * k] + piv);
            cf o1 = pfma(pfma(z[2 * k + 1], cf{a, a}, cf{am, am}), G[2 * k + 1], z[2 * k + 1] + piv);
            *(f32x4*)(op + (k >> 1) * 32 + (k & 1) * 4) = f32x4{o0.x, o0.y, o1.x, o1.y};
          }
        }
      }
    }
    if (L == 0) {
      BAR();
      STAMP(9);
    }
  }
  if (me == 25 * 9 && tid == 0) st8(tag, rs, 0, 0);
  STAMP(16);
  DUMP();
}

extern "C" void kernel_launch(void* const* d_in, const int* in_sizes, int n_in, void* d_out, int out_size, void* d_ws,
                              size_t ws_size, hipStream_t stream) {
  const u16* const* in = (const u16* const*)d_in;
  fused_kernel<<<256, 512, 0, stream>>>(in[0], in[1], in[2], in[3], in[4], in[5], in[6], in[7], in[8], in[10], in[11],
                                        in[12], in[13], in[14], (float*)d_out, (char*)d_ws);
}
```

```cpp
#include <hip/hip_runtime.h>
#include <hip/hip_bf16.h>
#include <stdint.h>

typedef __bf16 bf16x8 __attribute__((ext_vector_type(8)));
typedef __bf16 bf16x2 __attribute__((ext_vector_type(2)));
typedef float f32x4 __attribute__((ext_vector_type(4)));
typedef float f32x2 __attribute__((ext_vector_type(2)));
typedef unsigned u32x4 __attribute__((ext_vector_type(4)));
typedef unsigned u32x2 __attribute__((ext_vector_type(2)));
typedef unsigned short u16;
typedef __amdgpu_buffer_rsrc_t rsrc_t;
#define DEV __device__ __forceinline__

constexpr int SEQ = 2048, DM = 576, TC = 80, NP = 26, NG = 9;
constexpr unsigned WS_KVF = 4096;
constexpr unsigned WS_GF = WS_KVF + 9 * 256;
constexpr unsigned WS_BLK = 32768;
constexpr unsigned BLK = 4352, BLK_S1 = 1024, BLK_S2 = 3584, BLK_CH = 264;
constexpr unsigned WS_RKV = WS_BLK + 234 * BLK;
constexpr unsigned WS_GT = WS_RKV + DM * 4096;
constexpr unsigned WS_HT = WS_GT + DM * 4096;
constexpr unsigned WS_XT = WS_HT + 2 * 234 * 20480;
constexpr unsigned WS_STAMP = WS_XT + 234 * 10240;
constexpr unsigned WS_W2 = WS_STAMP + 65536;
constexpr unsigned WS_END = WS_W2 + 81 * 16384;
constexpr unsigned RB = DM * 2, OOB = 0x80000000u;
constexpr int L_K = 92160, L_R = 0, L_X = 16384, L_P = 40960, L_H = 81920, L_B = 122880, L_G = 143360, L_M = 158720;
constexpr int SPIN = 1 << 16;
constexpr unsigned SENT = 0xFFFFFFFFu;

#define BAR() asm volatile("s_waitcnt lgkmcnt(0)\n\ts_barrier" ::: "memory")
#define DRAIN() asm volatile("s_waitcnt vmcnt(0)" ::: "memory")
#define STAMP(i)
#define DUMP()

DEV unsigned pk(float lo, float hi) { f32x2 v = {lo, hi}; return __builtin_bit_cast(unsigned, __builtin_convertvector(v, bf16x2)); }
DEV float lo16(unsigned u) { return __uint_as_float(u << 16); }
DEV float hi16(unsigned u) { return __uint_as_float(u & 0xffff0000u); }
DEV float rb(float x) { return lo16(pk(x, x)); }
DEV float b2f(u16 h) { return __uint_as_float((unsigned)h << 16); }
template <int C> DEV float dpp(float v) { return __int_as_float(__builtin_amdgcn_update_dpp(0, __float_as_int(v), C, 0xf, 0xf, true)); }
DEV float dot2(unsigned a, unsigned b, float c) { return __builtin_amdgcn_fdot2_f32_bf16(__builtin_bit_cast(bf16x2, a), __builtin_bit_cast(bf16x2, b), c, false); }
DEV f32x4 mfma(u32x4 a, u32x4 b, f32x4 c) { return __builtin_amdgcn_mfma_f32_16x16x32_bf16(__builtin_bit_cast(bf16x8, a), __builtin_bit_cast(bf16x8, b), c, 0, 0, 0); }
DEV unsigned canon(float v) { unsigned u = __float_as_uint(v); return u == SENT ? 0x7fc00000u : u; }
typedef f32x2 cf;
DEV cf pfma(cf a, cf b, cf c) { return __builtin_elementwise_fma(a, b, c); }
DEV cf cfma(cf a, cf b, cf c) { return pfma(cf{-a.y, a.y}, b.yx, pfma(a.xx, b, c)); }
DEV cf cmul(cf a, cf b) { return pfma(cf{-a.y, a.y}, b.yx, a.xx * b); }
DEV cf csel(bool p, cf a, cf b) { return {p ? a.x : b.x, p ? a.y : b.y}; }
DEV cf up2(unsigned u) { return {lo16(u), hi16(u)}; }
DEV unsigned pk2(cf v) { return __builtin_bit_cast(unsigned, __builtin_convertvector(v, bf16x2)); }
DEV cf sw16(float v) { return __builtin_bit_cast(cf, __builtin_amdgcn_permlane16_swap(__float_as_uint(v), __float_as_uint(v), false, false)); }
DEV cf sw32(float v) { return __builtin_bit_cast(cf, __builtin_amdgcn_permlane32_swap(__float_as_uint(v), __float_as_uint(v), false, false)); }
DEV float red16(float v) { cf t = sw16(v); return t.x + t.y; }
DEV float red32(float v) { cf t = sw32(v); return t.x + t.y; }
#define PIN(x) asm volatile("" : "+v"(x))
#define FENCE() do { asm volatile("" ::: "memory"); __builtin_amdgcn_sched_barrier(0); } while (0)
DEV rsrc_t mkrs(const void* p, unsigned n) { return __builtin_amdgcn_make_buffer_rsrc((void*)p, 0, n, 0x00020000); }
DEV u32x4 in16(rsrc_t rs, unsigned v, unsigned s) { return __builtin_amdgcn_raw_buffer_load_b128(rs, v, s, 0); }
DEV unsigned in4(rsrc_t rs, unsigned v) { return __builtin_amdgcn_raw_buffer_load_b32(rs, v, 0, 0); }
DEV u32x4 ld16(rsrc_t rs, unsigned v, unsigned s) { return __builtin_amdgcn_raw_buffer_load_b128(rs, v, s, 16); }
DEV u32x2 ld8(rsrc_t rs, unsigned v, unsigned s) { return __builtin_amdgcn_raw_buffer_load_b64(rs, v, s, 16); }
DEV unsigned ld4(rsrc_t rs, unsigned v, unsigned s) { return __builtin_amdgcn_raw_buffer_load_b32(rs, v, s, 16); }
template <int AUX = 16> DEV void st16(u32x4 d, rsrc_t rs, unsigned v, unsigned s) {
  __builtin_amdgcn_raw_buffer_store_b128(d, rs, v, s, AUX);
  asm volatile("s_nop 1" :: "v"(d));
}
DEV void st8(u32x2 d, rsrc_t rs, unsigned v, unsigned s) { __builtin_amdgcn_raw_buffer_store_b64(d, rs, v, s, 16); }
DEV void st4(unsigned d, rsrc_t rs, unsigned v, unsigned s) { __builtin_amdgcn_raw_buffer_store_b32(d, rs, v, s, 16); }
DEV int wrap9(int v) { return v >= 9 ? v - 9 : v; }
DEV int gof(int g, int rot, int i) { return i == 0 ? g : wrap9(g + 1 + ((i - 1 + rot) & 7)); }
DEV int fsw(int row) { return ((row / 20) << 1) | ((row >> 1) & 1); }

DEV void poll_flags(rsrc_t rs, unsigned off, unsigned vo, u32x2 tag) {
#pragma unroll 1
  for (int it = 0; it < SPIN; ++it) {
    u32x2 v = ld8(rs, vo, off);
    if (__all(v.x == tag.x && v.y == tag.y)) break;
    asm volatile("s_sleep 1" ::: "memory");
  }
  asm volatile("" ::: "memory");
}

DEV void build_k(char* lds, int tid, u32x4 lo, u32x4 hi) {
  unsigned w[8] = {lo.x, lo.y, lo.z, lo.w, hi.x, hi.y, hi.z, hi.w}, o[7];
#pragma unroll
  for (int k = 0; k < 7; ++k) o[k] = __builtin_amdgcn_alignbit(w[k + 1], w[k], 16);
  char* p = lds + L_K + (tid >> 8) * 33280 + ((tid & 255) + 1) * 16;
#pragma unroll
  for (int s = 0; s < 8; ++s) {
    u32x4 v;
    if (s & 1) v = u32x4{w[(s + 1) / 2], w[(s + 1) / 2 + 1], w[(s + 1) / 2 + 2], w[(s + 1) / 2 + 3]};
    else v = u32x4{o[s / 2], o[s / 2 + 1], o[s / 2 + 2], o[s / 2 + 3]};
    *(u32x4*)(p + s * 4160) = v;
  }
  if (tid < 16) *(u32x4*)(lds + L_K + (tid >> 3) * 33280 + (tid & 7) * 4160) = u32x4{0, 0, 0, 0};
}
DEV void load_k(const u16* ssk, int d0, int tid, u32x4& lo, u32x4& hi) {
  const u16* kr = ssk + (size_t)(d0 + (tid >> 8)) * SEQ;
  int ci = tid & 255;
  hi = *(const u32x4*)(kr + 8 * ci);
  lo = *(const u32x4*)(kr + 8 * (ci > 0 ? ci - 1 : 0));
}
template <int SET, class FN> DEV void conv_mfma(const char* kb, const char* ub, f32x4 (&Y)[4], FN side) {
  constexpr int C0 = SET ? 1 : 0, C1 = SET ? 2 : 3, C2 = SET ? 4 : 5, C3 = SET ? 7 : 6, N = C3 + 1;
  u32x4 U[N], K[3];
#pragma unroll
  for (int p = C3; p >= 0; --p) U[p] = *(const u32x4*)(ub + 512 * (7 - p));
#pragma unroll
  for (int a = 0; a < 3; ++a) K[a] = *(const u32x4*)(kb + 512 * a);
  __builtin_amdgcn_sched_barrier(0);
#pragma unroll
  for (int st = 0; st < 4 * N; ++st) {
    const int rr = st / N, a = st % N, nx = st + 3;
    Y[3] = mfma(K[st % 3], U[C3 - a], Y[3]);
    if (C2 >= a) Y[2] = mfma(K[st % 3], U[C2 >= a ? C2 - a : 0], Y[2]);
    if (C1 >= a) Y[1] = mfma(K[st % 3], U[C1 >= a ? C1 - a : 0], Y[1]);
    if (C0 >= a) Y[0] = mfma(K[st % 3], U[C0 >= a ? C0 - a : 0], Y[0]);
    if (nx < 4 * N) K[st % 3] = *(const u32x4*)(kb + 512 * (nx % N) + 64 * (nx / N));
    if (rr < 3) U[C3 - a] = *(const u32x4*)(ub + 512 * (7 - (C3 - a)) + 64 * (rr + 1));
    side(st);
    __builtin_amdgcn_sched_barrier(0);
  }
}
template <class FN> DEV void conv_pair(char* lds, rsrc_t rs, int d0, int tid, int w, u32x4 rv, float dd, FN mid) {
  int l = tid & 63, chn = w >> 2, set = w & 1, rh = (w >> 1) & 1;
  if ((tid & 255) < 32) *(u32x4*)(lds + L_R + chn * 4608 + 4096 + l * 16) = u32x4{0, 0, 0, 0};
  *(u32x4*)(lds + L_R + chn * 4608 + (tid & 255) * 16) = rv;
  BAR();
  STAMP(20);
  int b = l & 15, g = l >> 4;
  const char* kb = lds + L_K + chn * 33280 + (b & 7) * 4160 + (g + (b >> 3)) * 16 + rh * 256;
  const char* ub = lds + L_R + chn * 4608 + (254 - 2 * b + g) * 16 - 3584 + rh * 256;
  f32x4 Y[4] = {};
  if (set) conv_mfma<1>(kb, ub, Y, mid); else conv_mfma<0>(kb, ub, Y, mid);
  STAMP(21);
  mid(-1);
  STAMP(22);
  f32x4 keep[2];
#pragma unroll
  for (int k = 0; k < 2; ++k) {
    f32x4 snd;
#pragma unroll
    for (int m = 0; m < 4; ++m) {
      float a = Y[k][m], bb = Y[2 + k][m];
      asm volatile("" : "+v"(a), "+v"(bb));
      snd[m] = rh ? a : bb; keep[k][m] = rh ? bb : a;
    }
    *(f32x4*)(lds + L_X + w * 2048 + k * 1024 + l * 16) = snd;
  }
  BAR();
  STAMP(23);
  u32x2 og[2];
#pragma unroll
  for (int k = 0; k < 2; ++k) {
    f32x4 y = keep[k] + *(const f32x4*)(lds + L_X + (w ^ 2) * 2048 + k * 1024 + l * 16);
    int cb = ((set ? 0x7421 : 0x6530) >> (4 * (2 * rh + k))) & 15;
    int tb = 256 * cb + 16 * b + 4 * g;
    u32x2 kv = *(const u32x2*)(lds + L_R + chn * 4608 + (2044 - tb) * 2);
    unsigned y01 = pk(y.x, y.y), y23 = pk(y.z, y.w);
    unsigned s01 = pk(hi16(kv.y) * dd, lo16(kv.y) * dd), s23 = pk(hi16(kv.x) * dd, lo16(kv.x) * dd);
    og[k] = u32x2{pk(lo16(y01) + lo16(s01), hi16(y01) + hi16(s01)), pk(lo16(y23) + lo16(s23), hi16(y23) + hi16(s23))};
  }
  u32x2 sx = __builtin_amdgcn_permlane16_swap(og[0].x, og[1].x, false, false), sy = __builtin_amdgcn_permlane16_swap(og[0].y, og[1].y, false, false);
  int cb = ((set ? 0x7421 : 0x6530) >> (4 * (2 * rh + (g & 1)))) & 15;
  st16(u32x4{sx.x, sy.x, sx.y, sy.y}, rs, 512 * cb + 32 * b + 16 * (g >> 1), WS_GT + (d0 + chn) * 4096);
}
constexpr int RG = 4;
template <int KIND> DEV u32x4 frag(const char* lds, int fb, int s) {
  if (KIND == 1) return *(const u32x4*)(lds + (fb ^ (((s / 5) & 1) * 64)) + (s / 10) * 10240 + (s % 5) * 512);
  return *(const u32x4*)(lds + (fb ^ (((s / 5) & 1) * 16)) + ((s / 10 + 5) % 6 + (s < 10 ? -1 : 0)) * 20480 + (s % 5) * 4096);
}
template <int KIND, int S0, int S1, class FN>
DEV void gsteps(const char* lds, int fb, const u32x4 (&W)[18], f32x4 (&acc)[5], u32x4 (&F)[RG + 1], FN side) {
#pragma unroll
  for (int s = S0; s < S1; ++s) {
    const int m = s - RG;
    if (s < 90) F[s % (RG + 1)] = frag<KIND>(lds, fb, s);
    __builtin_amdgcn_sched_barrier(0);
    if (m >= 0) acc[m % 5] = KIND == 1 ? mfma(F[m % (RG + 1)], W[m / 5], acc[m % 5]) : mfma(W[m / 5], F[m % (RG + 1)], acc[m % 5]);
    side(s);
    __builtin_amdgcn_sched_barrier(0);
  }
}
#define XS 64
#define DR 0
DEV void ldb(rsrc_t rs, u32x4* T, unsigned so, int l) {
#pragma unroll
  for (int n = 0; n < 5; ++n) T[n] = ld16(rs, l * 16 + n * 1024, so);
}
DEV void need(rsrc_t rs, u32x4* T, unsigned so, int l) {
#pragma unroll 1
  for (int it = 0; it < SPIN; ++it) {
    unsigned m = 0;
#pragma unroll
    for (int n = 0; n < 5; ++n) m = max(max(max(m, T[n].x), T[n].y), max(T[n].z, T[n].w));
    if (!__any(m == SENT)) break;
    asm volatile("s_sleep %0" :: "n"(DR) : "memory");
    ldb(rs, T, so, l);
  }
}
#define OAUX 16
#define FLS 34
#define GL 0
#define DS1 10
#define DS2 10
#define DE 15
#define DX 8
#define DH0 16
#define DH1 12
#define PINS(x) asm volatile("" : "+s"(x))

__global__ __launch_bounds__(512, 2) void fused_kernel(
    const u16* __restrict__ X, const u16* __restrict__ Lre, const u16* __restrict__ Lim, const u16* __restrict__ Bre,
    const u16* __restrict__ Bim, const u16* __restrict__ Cre, const u16* __restrict__ Cim, const u16* __restrict__ logdt,
    const u16* __restrict__ Dsk, const u16* __restrict__ Kin, const u16* __restrict__ Vin, const u16* __restrict__ shk,
    const u16* __restrict__ ssk, const u16* __restrict__ sD, float* __restrict__ out, char* ws) {
  __shared__ __attribute__((aligned(16))) char lds[163840];
  int tid = threadIdx.x, l = tid & 63;
  const int w = __builtin_amdgcn_readfirstlane(tid >> 6);
  const int xcd = blockIdx.x & 7, loc = blockIdx.x >> 3;
  int c, g, spare = -1;
  if (loc < 27) { c = 3 * xcd + loc / 9; g = loc % 9; }
  else {
    int idx = xcd * 5 + loc - 27;
    if (idx >= 36) return;
    if (idx < 18) { c = 24 + idx / 9; g = idx % 9; } else { spare = idx - 18; c = 0; g = spare >> 1; }
  }
  asm volatile("" : "+s"(ws));
  const rsrc_t rs = __builtin_amdgcn_make_buffer_rsrc(ws, 0, WS_END, 0x00020000);
  for (int i = 30; i >= 0; --i) STAMP(i);
#define GETTAG() do { tag.x = __builtin_amdgcn_readfirstlane(tag.x) + 1; tag.y = __builtin_amdgcn_readfirstlane(tag.y) + 1; } while (0)
  if (spare >= 0) {
    u32x2 tag = ld8(rs, 0, 0);
    const int d0 = 64 * g + 2 * (26 + 3 * (spare & 1));
    u32x4 klo[3], khi[3], rv[3];
    {
      const rsrc_t r0 = mkrs(Cre, DM * RB), r1 = mkrs(Cim, DM * RB);
      const unsigned dro = (64 * g + 16 * (w & 3) + (l & 15)) * RB + 64 * (w >> 2) + 16 * (l >> 4);
      const int i0 = 4 * (spare & 1);
      u32x4 re[5], im[5];
#pragma unroll
      for (int i = 0; i < 5; ++i) { re[i] = in16(r0, dro, (i0 + i) * 128); im[i] = in16(r1, dro, (i0 + i) * 128); }
      FENCE();
#pragma unroll
      for (int r = 0; r < 3; ++r) load_k(ssk, d0 + 2 * r, tid, klo[r], khi[r]);
      FENCE();
#pragma unroll
      for (int i = 0; i < 5; ++i)
#pragma unroll
        for (int k = 0; k < 2; ++k) {
          unsigned a = re[i][2 * k], b = im[i][2 * k] ^ 0x80008000u, a2 = re[i][2 * k + 1], b2 = im[i][2 * k + 1] ^ 0x80008000u;
          u32x4 o = {__builtin_amdgcn_perm(b, a, 0x05040100), __builtin_amdgcn_perm(b, a, 0x07060302),
                     __builtin_amdgcn_perm(b2, a2, 0x05040100), __builtin_amdgcn_perm(b2, a2, 0x07060302)};
          if (i > 0 || i0 == 0) st16(o, rs, l * 16 + k * 1024, WS_W2 + ((g * 9 + i0 + i) * 8 + w) * 2048);
        }
    }
    GETTAG();
#pragma unroll
    for (int r = 0; r < 3; ++r) { PIN(klo[r]); PIN(khi[r]); }
    DRAIN();
    BAR();
    if (tid == 0) st8(tag, rs, 0, WS_KVF + g * 256 + (NP + (spare & 1)) * 8);
#pragma unroll 1
    for (int r = 0; r < 3; ++r) {
      if ((tid & 255) == 0) klo[0] = u32x4{0, 0, 0, 0};
      build_k(lds, tid, klo[0], khi[0]);
      if (r == 0) {
        poll_flags(rs, WS_KVF + g * 256, min(l, NP - 1) * 8, tag);
#pragma unroll
        for (int k = 0; k < 3; ++k) rv[k] = ld16(rs, (tid & 255) * 16, WS_RKV + (d0 + 2 * k + (w >> 2)) * 4096);
      }
      conv_pair(lds, rs, d0 + 2 * r, tid, w, rv[0], b2f(sD[d0 + 2 * r + (w >> 2)]), [](int) {});
      klo[0] = klo[1]; klo[1] = klo[2]; khi[0] = khi[1]; khi[1] = khi[2]; rv[0] = rv[1]; rv[1] = rv[2];
      BAR();
    }
    DRAIN();
    BAR();
    if (tid == 0) st8(tag, rs, 0, WS_GF + g * 256 + (26 + (spare & 1)) * 8);
    STAMP(1);
    DUMP();
    return;
  }
  asm volatile("" ::: "memory");
  u32x2 tag = ld8(rs, 0, 0);
  const int t0 = TC * c, me = c * 9 + g, rot = 3 * (c % 3);
  int q = l >> 4, ri = l & 1, pp = (l & 15) >> 1, s8 = 2 * q + ri, er, ei, eo0;
#define REDERIVE() do { asm volatile("" : "+v"(tid)); l = tid & 63; q = l >> 4; ri = l & 1; pp = (l & 15) >> 1; s8 = 2 * q + ri; \
    er = tid >> 2; ei = l & 3; eo0 = er * 128 + ((ei ^ fsw(er)) << 4); } while (0)
  const u32x4 S4 = {SENT, SENT, SENT, SENT};
  auto clr = [&](unsigned so, int n) {
#pragma unroll
    for (int k = 0; k < n; k += 512) st16(S4, rs, k + 512 <= n || tid < n - k ? tid * 16 : OOB, so + k * 16);
  };
  const rsrc_t rx = mkrs(X, SEQ * RB);
  unsigned kk[11], vv[8]; u32x4 shv;
  const int dp = l & 31, tb = t0 + 8 * (2 * w + (l >> 5));
  if (w < 5) {
    const rsrc_t rk = mkrs(Kin, SEQ * RB), rvv = mkrs(Vin, SEQ * RB);
    unsigned b1 = tb * RB + 128 * g + 4 * dp, b0 = tb ? b1 - 3 * RB : OOB, b2 = b1 + 4 * RB;
    PIN(b0); PIN(b1); PIN(b2);
    shv = *(const u32x4*)(shk + (64 * g + 2 * dp) * 4);
#pragma unroll
    for (int j = 0; j < 11; ++j) kk[j] = in4(rk, j < 3 ? b0 + j * RB : j < 7 ? b1 + (j - 3) * RB : b2 + (j - 7) * RB);
#pragma unroll
    for (int j = 0; j < 8; ++j) vv[j] = in4(rvv, j < 4 ? b1 + j * RB : b2 + (j - 4) * RB);
  }
  FENCE();
  const unsigned po = w == 5 ? (64 * g + (l & 62)) * 2 : OOB;
  const unsigned pr[3] = {in4(mkrs(Lre, RB), po), in4(mkrs(Lim, RB), po), in4(mkrs(logdt, RB), po)};
  FENCE();
  u32x4 W1[18];
  auto ldw1 = [&](int i0) {
    const rsrc_t r0 = mkrs(Bre, DM * RB), r1 = mkrs(Bim, DM * RB);
    const unsigned wo = (64 * g + 8 * w + (l >> 3)) * RB + (l & 7) * 16;
#pragma unroll
    for (int i = i0; i < 9; ++i) { W1[2 * i] = in16(r0, wo, gof(g, rot, i) * 128); W1[2 * i + 1] = in16(r1, wo, gof(g, rot, i) * 128); }
  };
  auto fixw1 = [&](int i0, int base) {
    const int sw = base + (l >> 3) * 128 + (((l & 7) ^ ((l >> 4) * 2)) << 4);
    const int sr = base + ri * 1024 + pp * 128 + ((q ^ ((pp >> 1) * 2 + ri)) << 4);
#pragma unroll
    for (int i = i0; i < 9; ++i) {
      *(u32x4*)(lds + sw + (i & 3) * 2048) = W1[2 * i];
      *(u32x4*)(lds + (sw ^ 16) + (i & 3) * 2048 + 1024) = W1[2 * i + 1];
      FENCE();
      W1[2 * i] = *(const u32x4*)(lds + sr + (i & 3) * 2048);
      W1[2 * i + 1] = *(const u32x4*)(lds + (sr ^ 64) + (i & 3) * 2048);
      FENCE();
    }
  };
  ldw1(0);
  FENCE();
  u32x4 xa[9], xb[3];
  const int row = tid >> 3, row2 = 64 + (tid >> 5), e = tid & 31;
  {
    unsigned xo = (t0 + row) * RB + (tid & 7) * 16, xo2 = (t0 + row2) * RB + (e & 7) * 16;
#pragma unroll
    for (int i = 0; i < 9; ++i) xa[i] = in16(rx, xo, gof(g, rot, i) * 128);
    xb[0] = in16(rx, xo2 + gof(g, rot, e >> 3) * 128, 0);
    xb[1] = in16(rx, xo2 + gof(g, rot, (e >> 3) + 4) * 128, 0);
    xb[2] = in16(rx, e < 8 ? xo2 : OOB, gof(g, rot, 8) * 128);
  }
  u32x4 klo, khi;
  load_k(ssk, 64 * g + 2 * c, tid, klo, khi);
  clr(WS_BLK + me * BLK, BLK_CH);
  clr(WS_HT + me * 20480, 1280);
  FENCE();
  if (w < 5) {
    cf sh[4] = {{lo16(shv.x), lo16(shv.z)}, {hi16(shv.x), hi16(shv.z)}, {lo16(shv.y), lo16(shv.w)}, {hi16(shv.y), hi16(shv.w)}};
    cf P[8];
#pragma unroll
    for (int j = 0; j < 8; ++j) {
      cf a = up2(kk[j]) * sh[3];
#pragma unroll
      for (int tt = 1; tt < 4; ++tt) a = pfma(up2(kk[j + tt]), sh[3 - tt], a);
      P[j] = a * up2(vv[j]);
    }
    u32x4 o0, o1;
#pragma unroll
    for (int k = 0; k < 4; ++k) { o0[k] = pk(P[7 - 2 * k].x, P[6 - 2 * k].x); o1[k] = pk(P[7 - 2 * k].y, P[6 - 2 * k].y); }
    unsigned vo = tb < SEQ ? 2 * dp * 4096 + (2040 - tb) * 2 : OOB;
    st16(o0, rs, vo, WS_RKV + 64 * g * 4096);
    st16(o1, rs, vo, WS_RKV + (64 * g + 1) * 4096);
  } else if (w == 5) {
    float lre = ri ? hi16(pr[0]) : lo16(pr[0]), lim = ri ? hi16(pr[1]) : lo16(pr[1]), ldt = ri ? hi16(pr[2]) : lo16(pr[2]);
    float dt = rb(__expf(ldt)), ee = __expf(dt * lre), rev = __builtin_amdgcn_fractf(dt * lim * 0.15915494309189535f);
    cf lam = cf{__builtin_amdgcn_cosf(rev), __builtin_amdgcn_sinf(rev)} * ee;
    float den = 2.f * __builtin_amdgcn_rcpf(lre * lre + lim * lim), nr = lam.x - 1.f, ni = lam.y;
    cf co = {(nr * lre + ni * lim) * den, (ni * lre - nr * lim) * den};
    *(f32x4*)(lds + L_M + 1024 + l * 16) = f32x4{lam.x, lam.y, co.x, co.y};
  }
  STAMP(17);
  FENCE();
  float cs;
  {
    fixw1(0, L_K + w * 8192);
    f32x4 ca = {};
#pragma unroll
    for (int i = 0; i < 18; ++i) ca = mfma(u32x4{0x3f803f80u, 0x3f803f80u, 0x3f803f80u, 0x3f803f80u}, W1[i], ca);
    cs = ca.x;
    PIN(cs);
  }
  FENCE();
  {
    float s1 = 0.f, s2 = 0.f, u1 = 0.f, u2 = 0.f;
    char* d = lds + row * 128 + (((tid & 7) ^ fsw(row)) << 4);
#pragma unroll
    for (int i = 0; i < 9; ++i) {
      *(u32x4*)(d + i * 10240) = xa[i];
#pragma unroll
      for (int k = 0; k < 4; ++k) { s1 = dot2(xa[i][k], 0x3f803f80u, s1); s2 = dot2(xa[i][k], xa[i][k], s2); }
    }
    char* d2 = lds + row2 * 128 + (((e & 7) ^ fsw(row2)) << 4) + (e >> 3) * 10240;
#pragma unroll
    for (int n = 0; n < 3; ++n) {
      if (n < 2 || e < 8) *(u32x4*)(d2 + n * 40960) = xb[n];
#pragma unroll
      for (int k = 0; k < 4; ++k) { u1 = dot2(xb[n][k], 0x3f803f80u, u1); u2 = dot2(xb[n][k], xb[n][k], u2); }
    }
    s1 += dpp<0xB1>(s1); s2 += dpp<0xB1>(s2); u1 += dpp<0xB1>(u1); u2 += dpp<0xB1>(u2);
    s1 += dpp<0x4E>(s1); s2 += dpp<0x4E>(s2); u1 += dpp<0x4E>(u1); u2 += dpp<0x4E>(u2);
    s1 += dpp<0x141>(s1); s2 += dpp<0x141>(s2); u1 += dpp<0x141>(u1); u2 += dpp<0x141>(u2);
    u1 += dpp<0x140>(u1); u2 += dpp<0x140>(u2);
    u1 = red16(u1); u2 = red16(u2);
    bool ex = (tid & 31) == 1;
    float a1 = (ex ? u1 : s1) * (1.f / DM), a2 = (ex ? u2 : s2) * (1.f / DM);
    float rsd = __builtin_amdgcn_rcpf(rb(__builtin_amdgcn_sqrtf(rb(rb(fmaxf(a2 - a1 * a1, 0.f)) + 1e-5f))));
    if (ex || (tid & 7) == 0) *(f32x2*)(lds + L_M + (ex ? row2 : row) * 8) = f32x2{rsd, -rb(a1)};
  }
  STAMP(18);
  GETTAG();
  cf lam, co;
  DRAIN();
  BAR();
  if (tid == 0) st8(tag, rs, 0, WS_KVF + g * 256 + c * 8);
  STAMP(1);
  if ((tid & 255) == 0) klo = u32x4{0, 0, 0, 0};
  build_k(lds, tid, klo, khi);
  STAMP(19);
  u32x4 W2[18];
  constexpr int W2A = 5, DLY_X = DX, DLY_H0 = DH0, DLY_H1 = DH1;
  auto ldw2 = [&](int i0, int i1) {
#pragma unroll
    for (int i = i0; i < i1; ++i) {
      const unsigned so = WS_W2 + ((g * 9 + gof(g, rot, i)) * 8 + w) * 2048;
      W2[2 * i] = ld16(rs, l * 16, so); W2[2 * i + 1] = ld16(rs, l * 16 + 1024, so);
    }
  };

#pragma unroll
  for (int L = 0; L < 2; ++L) {
    f32x4 acc[5] = {};
    u32x4 F[RG + 1];
    REDERIVE();
    const int ab = (20 * ((l & 15) >> 2) + (l & 3)) * 128 + ((q ^ ((l & 15) >> 1)) << 4);
    cf l10, l20;
    if (L) fixw1(8, 102400 + w * 2048);
    { f32x4 t = *(const f32x4*)(lds + L_M + 1024 + (8 * w + pp) * 16); lam = t.xy; co = t.zw; }
    cf cA = csel(ri, cf{-co.y, co.x}, co), cB = csel(ri, co, cf{-co.y, co.x});
    PIN(cA); PIN(cB);
    {
      cf l2 = cmul(lam, lam), l4 = cmul(l2, l2), l5 = cmul(l4, lam);
      l10 = cmul(l5, l5); l20 = cmul(l10, l10);
    }
    PIN(l10); PIN(l20);
    cf CS;
    u32x4 rv;
    if (L == 0) {
      u32x2 fl;
      unsigned fa = g * 256, fb = c * 8;
      PINS(fa); PINS(fb);
      const unsigned fo = l < NP + 2 ? fa + l * 8 : min(l - NP - 2, 8) * 256 + fb;
      gsteps<1, 0, 90 + RG>(lds, ab, W1, acc, F, [&](int s) {
        if (s == 4) clr(WS_XT + me * 10240, 512);
        if (s == 9) clr(WS_XT + me * 10240 + 8192, 128);
        if (s == 14 || s == 20) clr(WS_HT + (234 + me) * 20480 + (s == 20) * 8192, 512);
        if (s == 26) clr(WS_HT + (234 + me) * 20480 + 16384, 256);
        if (s == 60) fl = ld8(rs, fo, WS_KVF);
      });
      STAMP(2);
      if (!__all(fl.x == tag.x && fl.y == tag.y)) poll_flags(rs, WS_KVF, fo, tag);
      STAMP(3);
      rv = ld16(rs, (tid & 255) * 16, WS_RKV + (64 * g + 2 * c + (w >> 2)) * 4096);
      FENCE();
      CS = pfma(cA, cf{cs, cs}, cB * dpp<0xB1>(cs));
    } else {
      u32x4 T[10];
      unsigned sA = WS_XT + (c * 9 + gof(g, rot, 1 + (w >> 1))) * 10240 + (w & 1) * 5120;
      unsigned sB = WS_XT + (c * 9 + gof(g, rot, 5 + (w >> 1))) * 10240 + (w & 1) * 5120;
      char* td = lds + w * 5120 + l * 16;
      gsteps<1, 0, 10>(lds, ab, W1, acc, F, [&](int s) {});
      asm volatile("s_sleep %0" :: "n"(DLY_X) : "memory");
      ldb(rs, T, sA, l); ldb(rs, T + 5, sB, l);
      need(rs, T, sA, l);
      STAMP(10);
#pragma unroll
      for (int n = 0; n < 5; ++n) *(u32x4*)(td + 10240 + n * 1024) = T[n];
      BAR();
      gsteps<1, 10, 50>(lds, ab, W1, acc, F, [&](int s) {
        if (s == 16) need(rs, T + 5, sB, l);
        if (s % 4 == 0 && s >= 16 && s < 36) *(u32x4*)(td + 51200 + (s / 4 - 4) * 1024) = T[5 + (s / 4 - 4) % 5];
      });
      BAR();
      gsteps<1, 50, 90 + RG>(lds, ab, W1, acc, F, [&](int s) {});
      STAMP(11);
    }
    REDERIVE();
    cf u[10], hin;
    {
#pragma unroll
      for (int j = 0; j < 10; ++j) {
        float lo = acc[j >> 2][j & 3], hi = acc[(10 + j) >> 2][(10 + j) & 3];
        float kept = ri ? hi : lo, rcv = dpp<0xB1>(ri ? lo : hi);
        if (L == 0) {
          cf ms = *(const cf*)(lds + L_M + (10 * s8 + j) * 8);
          u[j] = pfma(cA, cf{kept, kept}, pfma(cB, cf{rcv, rcv}, CS * ms.y)) * ms.x;
        } else {
          u[j] = pfma(cA, cf{kept, kept}, cB * rcv);
        }
      }
      cf h = u[0];
#pragma unroll
      for (int j = 1; j < 10; ++j) h = cfma(lam, h, u[j]);
      cf o = {dpp<0xB1>(h.x), dpp<0xB1>(h.y)};
      cf ev = csel(ri, o, h), od = csel(ri, h, o);
      cf P = cfma(l10, ev, od);
      cf ax = sw16(P.x), ay = sw16(P.y);
      cf Pe = {ax.x, ay.x}, Po = {ax.y, ay.y}, D = cfma(l20, Pe, Po);
      cf bx = sw32(D.x), by = sw32(D.y), S2 = {bx.x, by.x}, l40 = cmul(l20, l20);
      cf E = cfma(l40, S2, cf{bx.y, by.y});
      if (s8 == 0) st8(u32x2{canon(E.x), canon(E.y)}, rs, (8 * w + pp) * 8, WS_BLK + me * BLK + L * 512);
      cf Sb = csel(q & 2, S2, cf{0.f, 0.f}), Sq = csel(q & 1, cfma(l20, Sb, Pe), Sb);
      hin = csel(ri, cfma(l10, Sq, ev), Sq);
    }
    u32x2 ev[4];
    const unsigned evs = WS_BLK + g * BLK + L * 512;
    auto ldev = [&] {
      const unsigned evo = (c - 1 - s8) * (9 * BLK) + (8 * w + pp) * 8;
#pragma unroll
      for (int i = 0; i < 4; ++i) ev[i] = ld8(rs, evo - i * (72 * BLK), evs);
    };
    BAR();
    if (L == 0) {
      STAMP(26);
      conv_pair(lds, rs, 64 * g + 2 * c, tid, w, rv, b2f(sD[64 * g + 2 * c + (w >> 2)]), [&](int st) {
        if (st < 0) { REDERIVE(); ldev(); ldw2(W2A, 9); FENCE(); }
        else if (st % 4 == 1 && st / 4 < W2A) ldw2(st / 4, st / 4 + 1);
      });
      STAMP(4);
    } else {
      if (DE) asm volatile("s_sleep %0" :: "n"(DE) : "memory");
      ldev();
    }
    REDERIVE();
    {
      cf one = {1.f, 0.f}, l40 = cmul(l20, l20), l80 = cmul(l40, l40), l160 = cmul(l80, l80), l320 = cmul(l160, l160);
      cf l640 = cmul(l320, l320);
      cf l10s = cmul(cmul(csel(ri, l10, one), csel(q & 1, l20, one)), csel(q & 2, l40, one));
      cf l80s = cmul(cmul(csel(ri, l80, one), csel(q & 1, l160, one)), csel(q & 2, l320, one));
      int ha[10];
#pragma unroll
      for (int j = 0; j < 10; ++j) {
        int t = 10 * s8 + j;
        ha[j] = L_H + t * 256 + (((2 * w + (pp >> 2)) ^ (t & 15)) << 4) + (pp & 3) * 4;
        PIN(ha[j]);
      }
      PIN(l640); PIN(l10s); PIN(l80s);
#pragma unroll 1
      for (int it = 0; it < SPIN; ++it) {
        bool ok = true;
#pragma unroll
        for (int i = 0; i < 4; ++i) ok = ok && ev[i].x != SENT && ev[i].y != SENT;
        if (__all(ok)) break;
        asm volatile("s_sleep %0" :: "n"(DR) : "memory");
        ldev();
      }
      cf H = __builtin_bit_cast(cf, ev[3]);
#pragma unroll
      for (int i = 2; i >= 0; --i) H = cfma(l640, H, __builtin_bit_cast(cf, ev[i]));
      H = cmul(l80s, H);
      H.x += dpp<0xB1>(H.x); H.y += dpp<0xB1>(H.y);
      H = {red32(red16(H.x)), red32(red16(H.y))};
      hin = cfma(l10s, H, hin);
      STAMP(5 + 7 * L);
#pragma unroll
      for (int j = 0; j < 10; ++j) {
        hin = cfma(lam, hin, u[j]);
        *(unsigned*)(lds + ha[j]) = pk2(hin);
      }
    }
    if (L == 0) DRAIN();
    BAR();
    const unsigned hbase = WS_HT + (L * NP + c) * 9 * 20480;
#pragma unroll
    for (int n = 0; n < 3; ++n)
      if (n < 2 || w < 4) st16(*(const u32x4*)(lds + L_H + tid * 16 + n * 8192), rs, tid * 16 + n * 8192, hbase + g * 20480);
    if (L == 0 && tid == 448) st8(tag, rs, 0, WS_GF + g * 256 + c * 8);
    STAMP(6 + 7 * L);
    cf G[8], z[8];
    u32x4 xs[4];
    {
      f32x4 y[5] = {};
      REDERIVE();
      const int hb = (l & 15) * 256 + (((8 * (w >> 2) + 2 * q) ^ (l & 15)) << 4);
      u32x4 T[10], gv[2];
      u32x2 fl;
      unsigned so[4];
#pragma unroll
      for (int k = 0; k < 4; ++k) so[k] = hbase + gof(g, rot, 1 + 2 * k + (w >> 2)) * 20480 + (w & 3) * 5120;
      char* td = lds + w * 5120 + l * 16;
      gsteps<2, 0, 10>(lds, hb, W2, y, F, [&](int s) {});
      asm volatile("s_sleep %0" :: "n"(L ? DLY_H1 : DLY_H0) : "memory");
      ldb(rs, T, so[0], l); ldb(rs, T + 5, so[1], l);
      need(rs, T, so[0], l);
      STAMP(7 + 7 * L);
#pragma unroll
      for (int n = 0; n < 5; ++n) { *(u32x4*)(td + n * 1024) = T[n]; T[n] = ld16(rs, l * 16 + n * 1024, so[2]); }
      BAR();
      gsteps<2, 10, 30>(lds, hb, W2, y, F, [&](int s) {
        if (s == 12) need(rs, T + 5, so[1], l);
        if (s % 3 == 0 && s < 27) { const int n = (s / 3 - 4) % 5; *(u32x4*)(td + 40960 + n * 1024) = T[5 + n]; T[5 + n] = ld16(rs, l * 16 + n * 1024, so[3]); }
      });
      BAR();
      if (L == GL) STAMP(20);
      gsteps<2, 30, 50>(lds, hb, W2, y, F, [&](int s) {
        if (s == 33) need(rs, T, so[2], l);
        if (s % 3 == 0 && s >= 33 && s < 48) { const int n = (s / 3 - 11) % 5; *(u32x4*)(td + 81920 + n * 1024) = T[n]; }
        if (L == 0 && s == FLS) fl = ld8(rs, min(l, 27) * 8, WS_GF + g * 256);
      });
      if (L == GL) STAMP(21);
      if (L == 0) {
        if (!__all(fl.x == tag.x && fl.y == tag.y)) poll_flags(rs, WS_GF + g * 256, min(l, 27) * 8, tag);
#pragma unroll
        for (int n = 0; n < 2; ++n) {
          int id = min(tid + 512 * n, 639), dl = id / 10, ck = id - 10 * dl;
          gv[n] = ld16(rs, dl * 4096 + min(t0 + 8 * ck, SEQ - 8) * 2, WS_GT + 64 * g * 4096);
        }
      }
      BAR();
      if (L == GL) STAMP(22);
      gsteps<2, 50, 70>(lds, hb, W2, y, F, [&](int s) {
        if (s == 51) need(rs, T + 5, so[3], l);
        if (L == 0 && s == XS && w < 5) {
          const unsigned xo = (t0 + (tid >> 2)) * RB + 16 * (l & 3);
          xs[0] = in16(rx, xo, 128 * g); xs[1] = in16(rx, xo + 64, 128 * g);
          const rsrc_t rd = mkrs(Dsk, RB);
          xs[2] = in16(rd, 16 * (l & 3), 128 * g); xs[3] = in16(rd, 16 * (l & 3) + 64, 128 * g);
        }
        if (s % 3 == 0 && s < 66) { const int n = (s / 3 - 17) % 5; *(u32x4*)(td + n * 1024) = T[5 + n]; }
      });
      BAR();
      REDERIVE();
      if (L == GL) STAMP(23);
      gsteps<2, 70, 90 + RG>(lds, hb, W2, y, F, [&](int s) {});
#pragma unroll
      for (int tt = 0; tt < 5; ++tt)
        *(f32x4*)(lds + L_P + (w >> 2) * 21760 + (16 * tt + (l & 15)) * 272 + (16 * (w & 3) + 4 * q) * 4) = y[tt];
      if (L == 0) {
#pragma unroll
        for (int n = 0; n < 2; ++n) {
          int id = min(tid + 512 * n, 639), dl = id / 10;
          *(u32x4*)(lds + L_G + id * 16 + ((dl >> 3) & 3) * 32 + (dl >> 5) * 128) = gv[n];
        }
        {
          cf ms = *(const cf*)(lds + L_M + er * 8);
#pragma unroll
          for (int k = 0; k < 8; ++k) {
            cf x0 = up2(xs[k >> 2][k & 3]) + ms.y;
            z[k] = pfma(x0 * ms.x, up2(xs[2 + (k >> 2)][k & 3]), x0);
            PIN(z[k]);
          }
        }
        BAR();
      }
      {
        const char* gp = lds + L_G + 8 * ei * 160 + ei * 32 + er * 2;
        if (L == 1) {
#pragma unroll
          for (int k = 0; k < 4; ++k) { f32x4 b = *(const f32x4*)(lds + L_B + k * 5120 + tid * 16); z[2 * k] = b.xy; z[2 * k + 1] = b.zw; }
        }
#pragma unroll
        for (int k = 0; k < 8; ++k) {
          int o = (k >> 2) * (32 * 160 + 128) + (k & 3) * 320;
          G[k] = {b2f(*(const u16*)(gp + o)), b2f(*(const u16*)(gp + o + 160))};
        }
      }
      if (L == 1) BAR();
    }
    STAMP(8 + 7 * L);
    if (L == 0) { ldw1(8); FENCE(); }
    if (w < 5) {
      const char* pb = lds + L_P + er * 272 + ei * 32;
      const float piv = *(const float*)(lds + L_M + er * 8 + 4 - 4 * L);
#pragma unroll
      for (int k = 0; k < 4; ++k) {
        int o = (k >> 1) * 128 + (k & 1) * 16;
        f32x4 a = *(const f32x4*)(pb + o), b = *(const f32x4*)(pb + 21760 + o);
        z[2 * k] += a.xy + b.xy; z[2 * k + 1] += a.zw + b.zw;
      }
      if (L == 0) {
        cf S[8] = {};
#pragma unroll
        for (int k = 0; k < 8; ++k) {
          cf zg = z[k] * G[k];
          S[0] += z[k]; S[1] = pfma(z[k], z[k], S[1]); S[2] += zg; S[3] = pfma(z[k], zg, S[3]);
          S[4] = pfma(zg, zg, S[4]); S[5] = pfma(zg, G[k], S[5]); S[6] += G[k]; S[7] = pfma(G[k], G[k], S[7]);
        }
        float s[8], r[4];
#pragma unroll
        for (int k = 0; k < 8; ++k) s[k] = S[k].x + S[k].y;
#pragma unroll
        for (int k = 0; k < 4; ++k) { const int a = (k >> 1) * 4 + (k & 1); r[k] = ((ei & 1) ? s[a + 2] : s[a]) + dpp<0xB1>((ei & 1) ? s[a] : s[a + 2]); }
        float v0 = ((ei & 2) ? r[2] : r[0]) + dpp<0x4E>((ei & 2) ? r[0] : r[2]);
        float v1 = ((ei & 2) ? r[3] : r[1]) + dpp<0x4E>((ei & 2) ? r[1] : r[3]);
        st8(u32x2{canon(v0), canon(v1)}, rs, tid * 8, WS_BLK + me * BLK + BLK_S1);
        STAMP(24);
        u32x2 sv[8];
        if (DS1) asm volatile("s_sleep %0" :: "n"(DS1) : "memory");
#pragma unroll 1
        for (int it = 0; it < SPIN; ++it) {
          bool ok = true;
#pragma unroll
          for (int k = 0; k < 8; ++k) { sv[k] = ld8(rs, tid * 8, WS_BLK + (c * 9 + wrap9(g + 1 + k)) * BLK + BLK_S1); ok = ok && sv[k].x != SENT && sv[k].y != SENT; }
          if (__all(ok)) break;
          asm volatile("s_sleep %0" :: "n"(DR) : "memory");
        }
        STAMP(25);
        cf V = {v0, v1};
#pragma unroll
        for (int k = 0; k < 8; ++k) V += __builtin_bit_cast(cf, sv[k]);
        V *= 1.f / DM;
        float mu = dpp<0x00>(V.x), zz = dpp<0x00>(V.y), zG = dpp<0x55>(V.x), zzG = dpp<0x55>(V.y);
        float zzGG = dpp<0xAA>(V.x), zGG = dpp<0xAA>(V.y), G1 = dpp<0xFF>(V.x), G2 = dpp<0xFF>(V.y);
        float a = __builtin_amdgcn_rsqf(fmaxf(zz - mu * mu, 0.f) + 1e-5f);
        float mw = mu + a * (zG - mu * G1);
        float ww = zz + 2.f * a * (zzG - mu * zG) + a * a * (zzGG - 2.f * mu * zGG + mu * mu * G2);
        float a2 = __builtin_amdgcn_rsqf(fmaxf(ww - mw * mw, 0.f) + 1e-5f), am = -a * mu;
        u32x4 o[2];
#pragma unroll
        for (int k = 0; k < 8; ++k) {
          z[k] = pfma(pfma(z[k], cf{a, a}, cf{am, am}), G[k], z[k] - mw);
          G[k] = z[k] * a2;
          o[k >> 2][k & 3] = pk2(G[k]);
        }
        st16(o[0], rs, eo0, WS_XT + me * 10240);
        st16(o[1], rs, eo0 ^ 64, WS_XT + me * 10240);
        FENCE();
        *(float*)(lds + L_M + er * 8) = mw - piv;
        *(u32x4*)(lds + eo0) = o[0];
        *(u32x4*)(lds + (eo0 ^ 64)) = o[1];
#pragma unroll
        for (int k = 0; k < 4; ++k) {
          cf b0 = pfma(G[2 * k], up2(xs[2 + (k >> 1)][2 * (k & 1)]), z[2 * k]);
          cf b1 = pfma(G[2 * k + 1], up2(xs[2 + (k >> 1)][2 * (k & 1) + 1]), z[2 * k + 1]);
          *(f32x4*)(lds + L_B + k * 5120 + tid * 16) = f32x4{b0.x, b0.y, b1.x, b1.y};
        }
      } else {
        cf S0 = z[0], S1 = z[0] * z[0];
#pragma unroll
        for (int k = 1; k < 8; ++k) { S0 += z[k]; S1 = pfma(z[k], z[k], S1); }
        float s0 = S0.x + S0.y, s1 = S1.x + S1.y;
        s0 += dpp<0xB1>(s0); s1 += dpp<0xB1>(s1); s0 += dpp<0x4E>(s0); s1 += dpp<0x4E>(s1);
        if (ei == 0) st8(u32x2{canon(s0), canon(s1)}, rs, er * 8, WS_BLK + me * BLK + BLK_S2);
        u32x2 sv[2];
        if (DS2) asm volatile("s_sleep %0" :: "n"(DS2) : "memory");
        const unsigned so[2] = {er * 8 + wrap9(g + 1 + ei) * BLK, er * 8 + wrap9(g + 5 + ei) * BLK};
#pragma unroll 1
        for (int it = 0; it < SPIN; ++it) {
          bool ok = true;
#pragma unroll
          for (int k = 0; k < 2; ++k) { sv[k] = ld8(rs, so[k], WS_BLK + c * 9 * BLK + BLK_S2); ok = ok && sv[k].x != SENT && sv[k].y != SENT; }
          if (__all(ok)) break;
          asm volatile("s_sleep %0" :: "n"(DR) : "memory");
        }
        cf V = __builtin_bit_cast(cf, sv[0]) + __builtin_bit_cast(cf, sv[1]);
        V.x += dpp<0xB1>(V.x); V.y += dpp<0xB1>(V.y); V.x += dpp<0x4E>(V.x); V.y += dpp<0x4E>(V.y);
        float mu = (s0 + V.x) * (1.f / DM), a = __builtin_amdgcn_rsqf(fmaxf((s1 + V.y) * (1.f / DM) - mu * mu, 0.f) + 1e-5f), am = -a * mu;
        {
          const rsrc_t ro = mkrs(out, SEQ * DM * 4);
          const unsigned oo = (t0 + er) * (DM * 4) + 256 * g + 16 * ei;
          const bool hi = ei & 1;
#pragma unroll
          for (int h = 0; h < 2; ++h) {
            cf o[4];
#pragma unroll
            for (int k = 0; k < 4; ++k) o[k] = pfma(pfma(z[4 * h + k], cf{a, a}, cf{am, am}), G[4 * h + k], z[4 * h + k] + piv);
            float v0[4], v1[4];
#pragma unroll
            for (int k = 0; k < 4; ++k) {
              float pa = k & 1 ? o[k >> 1].y : o[k >> 1].x, pb = k & 1 ? o[2 + (k >> 1)].y : o[2 + (k >> 1)].x;
              float a0 = dpp<0x50>(pa), b0 = dpp<0x50>(pb), a1 = dpp<0xFA>(pa), b1 = dpp<0xFA>(pb);
              v0[k] = hi ? b0 : a0; v1[k] = hi ? b1 : a1;
            }
            st16<OAUX>(__builtin_bit_cast(u32x4, f32x4{v0[0], v0[1], v0[2], v0[3]}), ro, oo + h * 128, 0);
            st16<OAUX>(__builtin_bit_cast(u32x4, f32x4{v1[0], v1[1], v1[2], v1[3]}), ro, oo + h * 128 + 64, 0);
          }
        }
      }
    }
    if (L == 0) {
      BAR();
      STAMP(9);
    }
  }
  if (me == 25 * 9 && tid == 0) st8(tag, rs, 0, 0);
  STAMP(16);
  DUMP();
}

extern "C" void kernel_launch(void* const* d_in, const int* in_sizes, int n_in, void* d_out, int out_size, void* d_ws,
                              size_t ws_size, hipStream_t stream) {
  const u16* const* in = (const u16* const*)d_in;
  fused_kernel<<<256, 512, 0, stream>>>(in[0], in[1], in[2], in[3], in[4], in[5], in[6], in[7], in[8], in[10], in[11],
                                        in[12], in[13], in[14], (float*)d_out, (char*)d_ws);
}
```
